```python
import torch

def _softmax(x, axis=-1):
    return torch.softmax(x, dim=axis)

def _gelu(x):
    return torch.nn.functional.gelu(x, approximate='tanh')

def _silu(x):
    return torch.nn.functional.silu(x)

def _sigmoid(x):
    return torch.sigmoid(x)

def _layernorm(x, axis=-1, eps=1e-05):
    m = x.mean(dim=axis, keepdim=True)
    v = x.var(dim=axis, keepdim=True, unbiased=False)
    return (x - m) / torch.sqrt(v + eps)

def _scatter_add(tgt, idx, src, axis=0):
    return tgt.clone().index_add_(axis, idx, src)

def _segment_sum(src, seg, n):
    out = torch.zeros((n,) + src.shape[1:], dtype=src.dtype)
    return out.index_add_(0, seg, src)

def _pad(x, pw):
    flat = tuple((v for pair in reversed(pw) for v in pair))
    return torch.nn.functional.pad(x, flat)
kernel_name = 'standalone__video_video_swin__vision'

def setup_inputs():

    _w = lambda a: (lambda t: t.to(torch.bfloat16) if t.is_floating_point() else t)(torch.as_tensor(a))
    T = 4
    H = W = 224 // 7 * 7
    C, win = (96, 7)
    return {'x': _w((lambda *s: torch.randn(*s, dtype=torch.bfloat16) * 0.1)(1, T, H, W, C)), 'wqkv_t': _w((lambda *s: torch.randn(*s, dtype=torch.bfloat16) * 0.1)(C, 3 * C)), 'wo_t': _w((lambda *s: torch.randn(*s, dtype=torch.bfloat16) * 0.1)(C, C)), 'wqkv_s': _w((lambda *s: torch.randn(*s, dtype=torch.bfloat16) * 0.1)(C, 3 * C)), 'wo_s': _w((lambda *s: torch.randn(*s, dtype=torch.bfloat16) * 0.05)(C, C))}

def reference(x, wqkv_t, wo_t, wqkv_s, wo_s):
    B, T, H, W, C = x.shape
    win = 7
    scale = C ** (-0.5)
    h = _layernorm(x, axis=-1)
    ht = (lambda x, *d: x.permute(*d))(h, 0, 2, 3, 1, 4).reshape(B * H * W, T, C)
    qkv = ht @ wqkv_t
    q, k, v = (qkv[..., :C].clone(), qkv[..., C:2 * C].clone(), qkv[..., 2 * C:].clone())
    p = _softmax(torch.einsum('btc,bsc->bts', q, k) * scale, axis=-1)
    ot = torch.einsum('bts,bsc->btc', p, v) @ wo_t
    ot = (lambda x, *d: x.permute(*d))(ot.reshape(B, H, W, T, C), 0, 3, 1, 2, 4)
    x = x + ot
    h = _layernorm(x, axis=-1)
    nh, nw = (H // win, W // win)
    hs = h.reshape(B * T, nh, win, nw, win, C)
    hw = (lambda x, *d: x.permute(*d))(hs, 0, 1, 3, 2, 4, 5).reshape(B * T * nh * nw, win * win, C)
    qkv = hw @ wqkv_s
    q, k, v = (qkv[..., :C].clone(), qkv[..., C:2 * C].clone(), qkv[..., 2 * C:].clone())
    p = _softmax(torch.einsum('bqc,bkc->bqk', q, k) * scale, axis=-1)
    os = torch.einsum('bqk,bkc->bqc', p, v) @ wo_s
    os = (lambda x, *d: x.permute(*d))(os.reshape(B * T, nh, nw, win, win, C), 0, 1, 3, 2, 4, 5)
    os = os.reshape(B, T, H, W, C)
    return x + os
```

```cpp
#include <hip/hip_runtime.h>
#include <hip/hip_bf16.h>
namespace naive {
typedef unsigned short u16;
__device__ __forceinline__ float bf2f(u16 v) { return __uint_as_float(((unsigned)v) << 16); }
__device__ __forceinline__ u16 f2bf(float f) { unsigned u = __float_as_uint(f); u += 0x7fffu + ((u >> 16) & 1u); return (u16)(u >> 16); }
__device__ __forceinline__ float rb(float f) { return bf2f(f2bf(f)); }
constexpr int C = 96, T = 4, H = 224, W = 224;

template <int NT>
__device__ void attn_block(float (*xs)[C], float (*hs)[C], float (*q)[C], float (*k)[C], float (*v)[C], float (*p)[NT + 1],
                           const u16* wqkv, const u16* wo, int tid, int nth) {
  for (int t = tid; t < NT; t += nth) {
    float m = 0; for (int c = 0; c < C; ++c) m += xs[t][c]; m /= C;
    float vv = 0; for (int c = 0; c < C; ++c) { float d = xs[t][c] - m; vv += d * d; } vv /= C;
    float mb = rb(m), vb = rb(vv);
    float s = rb(sqrtf(rb(vb + 1e-5f)));
    for (int c = 0; c < C; ++c) hs[t][c] = rb(rb(xs[t][c] - mb) / s);
  }
  __syncthreads();
  for (int i = tid; i < NT * 3 * C; i += nth) {
    int t = i / (3 * C), d = i % (3 * C);
    float a = 0; for (int c = 0; c < C; ++c) a += hs[t][c] * bf2f(wqkv[c * 3 * C + d]);
    a = rb(a);
    if (d < C) q[t][d] = a; else if (d < 2 * C) k[t][d - C] = a; else v[t][d - 2 * C] = a;
  }
  __syncthreads();
  const float scale = 0.10206207261596577f;
  for (int i = tid; i < NT * NT; i += nth) {
    int a = i / NT, b = i % NT;
    float s = 0; for (int c = 0; c < C; ++c) s += q[a][c] * k[b][c];
    p[a][b] = rb(rb(s) * scale);
  }
  __syncthreads();
  for (int a = tid; a < NT; a += nth) {
    float m = -1e30f; for (int b = 0; b < NT; ++b) m = fmaxf(m, p[a][b]);
    float l = 0; for (int b = 0; b < NT; ++b) l += expf(p[a][b] - m);
    for (int b = 0; b < NT; ++b) p[a][b] = rb(expf(p[a][b] - m) / l);
  }
  __syncthreads();
  for (int i = tid; i < NT * C; i += nth) {
    int a = i / C, c = i % C;
    float s = 0; for (int b = 0; b < NT; ++b) s += p[a][b] * v[b][c];
    q[a][c] = rb(s);
  }
  __syncthreads();
  for (int i = tid; i < NT * C; i += nth) {
    int a = i / C, c = i % C;
    float s = 0; for (int e = 0; e < C; ++e) s += q[a][e] * bf2f(wo[e * C + c]);
    k[a][c] = rb(xs[a][c] + rb(s));
  }
  __syncthreads();
}

__global__ void __launch_bounds__(128) temporal(const u16* x, const u16* wqkv, const u16* wo, u16* x1) {
  __shared__ float xs[T][C], hs[T][C], q[T][C], k[T][C], v[T][C], p[T][T + 1];
  const int pos = blockIdx.x;
  for (int i = threadIdx.x; i < T * C; i += 128) xs[i / C][i % C] = bf2f(x[((size_t)(i / C) * H * W + pos) * C + i % C]);
  __syncthreads();
  attn_block<T>(xs, hs, q, k, v, p, wqkv, wo, threadIdx.x, 128);
  for (int i = threadIdx.x; i < T * C; i += 128) x1[((size_t)(i / C) * H * W + pos) * C + i % C] = f2bf(k[i / C][i % C]);
}
__global__ void __launch_bounds__(256) spatial(const u16* x1, const u16* wqkv, const u16* wo, u16* out) {
  extern __shared__ float sm[];
  float (*xs)[C] = (float (*)[C])sm;
  float (*hs)[C] = xs + 49; float (*q)[C] = hs + 49; float (*k)[C] = q + 49; float (*v)[C] = k + 49;
  float (*p)[50] = (float (*)[50])(v + 49);
  const int t = blockIdx.x / 1024, wy = (blockIdx.x / 32) % 32, wx = blockIdx.x % 32;
  for (int i = threadIdx.x; i < 49 * C; i += 256) { int n = i / C; int y = wy * 7 + n / 7, xx = wx * 7 + n % 7; xs[n][i % C] = bf2f(x1[(((size_t)t * H + y) * W + xx) * C + i % C]); }
  __syncthreads();
  attn_block<49>(xs, hs, q, k, v, p, wqkv, wo, threadIdx.x, 256);
  for (int i = threadIdx.x; i < 49 * C; i += 256) { int n = i / C; int y = wy * 7 + n / 7, xx = wx * 7 + n % 7; out[(((size_t)t * H + y) * W + xx) * C + i % C] = f2bf(k[n][i % C]); }
}
inline void launch(void* const* d_in, void* d_out, void* x1buf, hipStream_t stream) {
  temporal<<<H * W, 128, 0, stream>>>((const u16*)d_in[0], (const u16*)d_in[1], (const u16*)d_in[2], (u16*)x1buf);
  const int smem = (5 * 49 * C + 49 * 50) * 4;
  spatial<<<4096, 256, smem, stream>>>((const u16*)x1buf, (const u16*)d_in[3], (const u16*)d_in[4], (u16*)d_out);
}
}
extern "C" void kernel_launch(void* const* d_in, const int* in_sizes, int n_in, void* d_out, int out_size, void* d_ws, size_t ws_size, hipStream_t stream) {
  hipFuncSetAttribute((const void*)naive::spatial, hipFuncAttributeMaxDynamicSharedMemorySize, (5 * 49 * 96 + 49 * 50) * 4);
  naive::launch(d_in, d_out, d_ws, stream);
}
```

```cpp
#include <hip/hip_runtime.h>
#include <hip/hip_bf16.h>

namespace vs {

typedef __bf16 bf16x8 __attribute__((ext_vector_type(8)));
typedef __bf16 bf16x2 __attribute__((ext_vector_type(2)));
typedef short s16x4 __attribute__((ext_vector_type(4)));
typedef float f32x4 __attribute__((ext_vector_type(4)));
typedef float f32x2 __attribute__((ext_vector_type(2)));
typedef unsigned u32x4 __attribute__((ext_vector_type(4)));
typedef unsigned u32x2 __attribute__((ext_vector_type(2)));
#define AS3 __attribute__((address_space(3)))
#define DI __device__ __forceinline__
#if defined(__HIP_DEVICE_COMPILE__)
#define NOPK __attribute__((target("no-packed-fp32-ops")))
#else
#define NOPK
#endif
#define SB() __builtin_amdgcn_sched_barrier(0)
#define PRIO_MODE 0
#define PRIO_S1() do { if (PRIO_MODE == 2) __builtin_amdgcn_s_setprio(1); if (PRIO_MODE == 3) __builtin_amdgcn_s_setprio(0); if (PRIO_MODE == 4) { if (team == (n & 1)) __builtin_amdgcn_s_setprio(1); else __builtin_amdgcn_s_setprio(0); } } while (0)
#define PRIO_S2() do { if (PRIO_MODE == 2) __builtin_amdgcn_s_setprio(0); if (PRIO_MODE == 3) __builtin_amdgcn_s_setprio(1); } while (0)
#define SGB(m, n) __builtin_amdgcn_sched_group_barrier(m, n, 0)
#define REP_SGB(N, V) _Pragma("unroll") for (int q_ = 0; q_ < N; ++q_) { SGB(0x8, 1); SGB(0x2, V); }
#define ATT_SCHED(t) do { if ((t) == 0) { REP_SGB(12, 10) } else if ((t) == 1) { REP_SGB(30, 4) } else { REP_SGB(18, 7) } } while (0)
#define STAMP(k) do {} while (0)
#define WG_BARRIER() do { asm volatile("s_waitcnt lgkmcnt(0)" ::: "memory"); __builtin_amdgcn_s_barrier(); asm volatile("" ::: "memory"); } while (0)
DI float u2f(unsigned u) { return __builtin_bit_cast(float, u); }

constexpr int TOKB = 192;
constexpr int ROWB = 224 * TOKB;
constexpr int FRAMEB = 224 * ROWB;
constexpr int FS = 9216 + 64;
constexpr int XTEAM = 4 * FS;
constexpr int W_OFF = 0;
constexpr int X_OFF = 73728;
constexpr int SPX = X_OFF + 2 * XTEAM;
constexpr int SPG = SPX + 3072;
constexpr int SPU = SPG + 3072;
constexpr int R2_OFF = SPU + 3072;
constexpr int SPR = R2_OFF + 4096;
constexpr int PSC = SPR + 64;
constexpr int FLG = PSC + 1024;
constexpr int LDS_BYTES = FLG + 64;

constexpr float INV_C = 1.0f / 96.0f;
constexpr float EPS = 1e-5f;
constexpr float KS = 0.10206207261596577f * 1.4426950408889634f;
constexpr float NEG_BIG = -3.0e38f;

DI f32x4 mfma(u32x4 a, u32x4 b, f32x4 c) {
  return __builtin_amdgcn_mfma_f32_16x16x32_bf16(__builtin_bit_cast(bf16x8, a), __builtin_bit_cast(bf16x8, b), c, 0, 0, 0);
}
DI unsigned pk(float a, float b) { f32x2 v = {a, b}; return __builtin_bit_cast(unsigned, __builtin_convertvector(v, bf16x2)); }
DI u32x4 pack8(f32x4 a, f32x4 b) { return u32x4{pk(a[0], a[1]), pk(a[2], a[3]), pk(b[0], b[1]), pk(b[2], b[3])}; }
template <class T> DI T lds_ld(unsigned a) { return *(AS3 T*)a; }
template <class T> DI void lds_st(unsigned a, T v) { *(AS3 T*)a = v; }
DI u32x2 lds_tr(unsigned a) { return __builtin_bit_cast(u32x2, __builtin_amdgcn_ds_read_tr16_b64_v4i16((AS3 s16x4*)a)); }
template <int CTRL> DI float dpp(float v) { return __builtin_bit_cast(float, __builtin_amdgcn_update_dpp(0, __builtin_bit_cast(int, v), CTRL, 0xf, 0xf, true)); }
DI float max2(float a, float b) { return __builtin_elementwise_maximum(a, b); }
DI float max3(float a, float b, float c) { return __builtin_elementwise_maximum(__builtin_elementwise_maximum(a, b), c); }
DI float exp2_(float x) { return __builtin_amdgcn_exp2f(x); }
DI float xrow_max(float v) {
  unsigned u = __builtin_bit_cast(unsigned, v);
  auto a = __builtin_amdgcn_permlane16_swap(u, u, false, false);
  v = max2(u2f(a[0]), u2f(a[1]));
  u = __builtin_bit_cast(unsigned, v);
  auto b = __builtin_amdgcn_permlane32_swap(u, u, false, false);
  return max2(u2f(b[0]), u2f(b[1]));
}
DI float xrow_sum(float v) {
  unsigned u = __builtin_bit_cast(unsigned, v);
  auto a = __builtin_amdgcn_permlane16_swap(u, u, false, false);
  v = u2f(a[0]) + u2f(a[1]);
  u = __builtin_bit_cast(unsigned, v);
  auto b = __builtin_amdgcn_permlane32_swap(u, u, false, false);
  return u2f(b[0]) + u2f(b[1]);
}
DI float inrow_sum(float v) { v += dpp<0x128>(v); v += dpp<0x124>(v); v += dpp<0x122>(v); v += dpp<0x121>(v); return v; }
DI float inrow_max(float v) { v = max2(v, dpp<0x128>(v)); v = max2(v, dpp<0x124>(v)); v = max2(v, dpp<0x122>(v)); v = max2(v, dpp<0x121>(v)); return v; }

DI void flag_arrive(unsigned addr, int lane) {
  asm volatile("s_waitcnt lgkmcnt(0)" ::: "memory");
  if (lane == 0) __hip_atomic_fetch_add((AS3 unsigned*)addr, 1u, __ATOMIC_RELAXED, __HIP_MEMORY_SCOPE_WORKGROUP);
  asm volatile("" ::: "memory");
}
DI void flag_wait(unsigned addr, unsigned target) {
  asm volatile("" ::: "memory");
#pragma unroll 1
  for (int spin = 0; spin < (1 << 16); ++spin) {
    unsigned v = __hip_atomic_load((AS3 unsigned*)addr, __ATOMIC_RELAXED, __HIP_MEMORY_SCOPE_WORKGROUP);
    if ((unsigned)__builtin_amdgcn_readfirstlane(v) >= target) break;
    __builtin_amdgcn_s_sleep(1);
  }
  asm volatile("" ::: "memory");
}

#define NT_LOAD " nt"
DI void dma_group(unsigned v0, unsigned v1, unsigned v2, const char* f0, const char* f1, const char* f2, const char* f3, unsigned xb) {
  unsigned keep;
#define D1(V, F, OFF) "s_add_u32 m0, %8, " #OFF "\n\ts_nop 0\n\tglobal_load_lds_dwordx4 " V ", " F NT_LOAD "\n\t"
  asm volatile(
      "s_mov_b32 %0, m0\n\t"
      "s_mov_b32 exec_hi, 0xffff\n\t"
      D1("%1", "%4", 0) D1("%1", "%5", 9280) D1("%1", "%6", 18560) D1("%1", "%7", 27840)
      D1("%2", "%4", 768) D1("%2", "%5", 10048) D1("%2", "%6", 19328) D1("%2", "%7", 28608)
      D1("%3", "%4", 1536) D1("%3", "%5", 10816) D1("%3", "%6", 20096) D1("%3", "%7", 29376)
      "s_mov_b32 exec_hi, -1\n\t"
      "s_mov_b32 m0, %0"
      : "=&s"(keep)
      : "v"(v0), "v"(v1), "v"(v2), "s"(f0), "s"(f1), "s"(f2), "s"(f3), "s"(xb)
      : "memory", "scc");
#undef D1
}
DI void dma_1k(unsigned voff, const char* base, unsigned ldsaddr) {
  unsigned keep;
  asm volatile("s_mov_b32 %0, m0\n\ts_mov_b32 m0, %3\n\ts_nop 0\n\tglobal_load_lds_dwordx4 %1, %2\n\ts_mov_b32 m0, %0"
               : "=&s"(keep) : "v"(voff), "s"(base), "s"(ldsaddr) : "memory");
}
template <int OFF> DI void gload16(u32x4& d, unsigned voff, const char* base) {
  asm volatile("global_load_dwordx4 %0, %1, %2 offset:%3" : "=v"(d) : "v"(voff), "s"(base), "i"(OFF) : "memory");
}

#define ST_AUX 0
DI void bstore(__amdgpu_buffer_rsrc_t rs, u32x4 v, unsigned voff) { __builtin_amdgcn_raw_buffer_store_b128(v, rs, voff, 0, ST_AUX); }
DI int chan(int ct, int i) { return 32 * (ct >> 1) + 8 * (i >> 2) + 4 * (ct & 1) + (i & 3); }
DI unsigned swz(int pos) { return 2u * ((pos ^ (pos >> 2)) & 1); }

DI float rstd(float q2, float s1) {
  const float m = s1 * INV_C;
  float v = fmaf(q2, INV_C, EPS);
  v = fmaf(-m, m, v);
  return __builtin_amdgcn_rsqf(fmaxf(v, EPS));
}
DI float sel4(f32x4 v, int k) {
  float a = (k & 1) ? v[1] : v[0], b = (k & 1) ? v[3] : v[2];
  return (k & 2) ? b : a;
}

DI u32x2 softmax_t(f32x4 S, float r, unsigned maskP) {
  const float r0 = dpp<0x00>(r), r1 = dpp<0x55>(r), r2 = dpp<0xAA>(r), r3 = dpp<0xFF>(r);
  const float t0 = S[0] * r0, t1 = S[1] * r1, t2 = S[2] * r2, t3 = S[3] * r3;
  const float mx = max3(max2(t0, t1), t2, t3);
  const float c = r * KS, mn = -mx * c;
  const float e0 = exp2_(fmaf(t0, c, mn)), e1 = exp2_(fmaf(t1, c, mn)), e2 = exp2_(fmaf(t2, c, mn)), e3 = exp2_(fmaf(t3, c, mn));
  const float inv = __builtin_amdgcn_rcpf((e0 + e1) + (e2 + e3));
  return u32x2{pk(e0 * r0 * inv, e1 * r1 * inv) & maskP, pk(e2 * r2 * inv, e3 * r3 * inv) & maskP};
}

#define WFRAG(m, ct, s) lds_ld<u32x4>(wl + ((((m) & WMASK) * 6 + (ct)) * 3 + (s)) * 1024)

template <bool ISA>
DI void prep_products(const u32x4 (&Lf)[6][3], const u32x4 (&Rf)[3][3], f32x4 (&D)[3][6], int c) {
#pragma unroll
  for (int rt = 0; rt < 6; ++rt) {
    f32x4 a = {0.f, 0.f, 0.f, 0.f};
#pragma unroll
    for (int s = 0; s < 3; ++s) a = mfma(Lf[rt][s], Rf[c][s], a);
    D[c][rt] = a;
  }
}
template <bool ISA>
DI void prep_finish(f32x4 (&D)[3][6], unsigned lb, int team, int half, int lane) {
  const int i = lane & 15, g = lane >> 4;
#pragma unroll
  for (int c = 0; c < 3; ++c) {
    float s = 0.f;
#pragma unroll
    for (int rt = 0; rt < 6; ++rt) s += (D[c][rt][0] + D[c][rt][1]) + (D[c][rt][2] + D[c][rt][3]);
    s = xrow_sum(s) * INV_C;
#pragma unroll
    for (int rt = 0; rt < 6; ++rt) D[c][rt] -= s;
  }
  if (ISA) {
    const unsigned ex = lb + SPG + team * 768;
#pragma unroll
    for (int rt = 0; rt < 6; ++rt) {
      f32x4 p = D[0][rt] + D[1][rt] + D[2][rt];
#pragma unroll
      for (int e = 0; e < 4; ++e) p[e] = inrow_sum(p[e]);
      if (i == 0) lds_st<f32x4>(ex + half * 384 + rt * 64 + g * 16, p);
    }
    flag_arrive(lb + FLG + 36 + team * 4, lane);
    flag_wait(lb + FLG + 36 + team * 4, 2);
#pragma unroll
    for (int rt = 0; rt < 6; ++rt) {
      const f32x4 b = (lds_ld<f32x4>(ex + rt * 64 + g * 16) + lds_ld<f32x4>(ex + 384 + rt * 64 + g * 16)) * INV_C;
#pragma unroll
      for (int c = 0; c < 3; ++c) D[c][rt] -= b;
    }
  }
  const int m = 2 * team + (ISA ? 0 : 1);
#pragma unroll
  for (int c = 0; c < 3; ++c)
#pragma unroll
    for (int s = 0; s < 3; ++s)
      lds_st<u32x4>(lb + W_OFF + ((m * 6 + 3 * half + c) * 3 + s) * 1024 + lane * 16, pack8(D[c][2 * s], D[c][2 * s + 1]));
}

DI void dma_matrix(const unsigned (&ve)[3], const unsigned (&vo)[3], const char* src, unsigned blkstride, unsigned dst) {
#pragma unroll
  for (int a = 0; a < 6; ++a) {
    const char* b = src + (size_t)a * blkstride;
#pragma unroll
    for (int k = 0; k < 3; ++k) dma_1k((a & 1) ? vo[k] : ve[k], b, dst + (3 * a + k) * 1024);
  }
}

NOPK __global__ void __launch_bounds__(512, 2)
swin_kernel(const char* __restrict__ x, const char* __restrict__ wqkv_t, const char* __restrict__ wo_t,
            const char* __restrict__ wqkv_s, const char* __restrict__ wo_s, char* __restrict__ out, char* __restrict__ dbg) {
  __shared__ __attribute__((aligned(256))) char lds_raw[LDS_BYTES];
  const unsigned lb = (unsigned)(size_t)(AS3 char*)lds_raw;
  const int lane = threadIdx.x & 63;
  const int wave = __builtin_amdgcn_readfirstlane(threadIdx.x >> 6);
  const int team = wave >> 2, w = wave & 3;
  const int i = lane & 15, g = lane >> 4;
  const int wy = blockIdx.x >> 3, wx0 = (blockIdx.x & 7) * 4;
  const char* xrow = x + (size_t)(wy * 7) * ROWB + (size_t)(wx0 * 7) * TOKB;
  char* orow = out + (size_t)(wy * 7) * ROWB + (size_t)(wx0 * 7) * TOKB;
  const unsigned xt = lb + X_OFF + team * XTEAM;

  unsigned dv[3];
  {
    const int l48 = lane < 48 ? lane : 47;
    const int pp = l48 / 12, ch = l48 % 12;
#pragma unroll
    for (int j = 0; j < 3; ++j) {
      const int pos = 12 * w + 4 * j + pp;
      dv[j] = ((pos / 7) * 224 + pos % 7) * TOKB + (ch ^ swz(pos)) * 16;
    }
  }
  const unsigned xb = xt + 12 * w * TOKB;

  STAMP(0);
  if (PRIO_MODE == 1 && team == 1) __builtin_amdgcn_s_setprio(1);
  {
    const char* wq = team ? wqkv_s : wqkv_t;
    const char* wo = team ? wo_s : wo_t;
    if (threadIdx.x < 16) lds_st<unsigned>(lb + FLG + threadIdx.x * 4, 0u);
    {
      unsigned ve[3], vo[3];
      const unsigned rstride = w < 3 ? 576 : 192;
#pragma unroll
      for (int k = 0; k < 3; ++k) {
        const int nn = 64 * k + lane, row = nn / 12, ch = nn % 12;
        ve[k] = row * rstride + ch * 16;
        vo[k] = row * rstride + (w < 3 ? ch ^ 3 : ch) * 16;
      }
      const char* src = w < 3 ? wq + w * 192 : wo;
      const unsigned dst = w < 2 ? lb + W_OFF + (2 * team + w) * 18432 : xt + (w - 2) * 18432;
      dma_matrix(ve, vo, src, 16 * rstride, dst);
    }
    if (wave == 4) {
#pragma unroll
      for (int k = 0; k < 3; ++k) {
        const int nn = 64 * k + lane, tok = nn / 12, ch = nn % 12;
        dma_1k((tok & 3) * FRAMEB + 6 * ROWB + ((tok >> 2) * 7 + 6) * TOKB + ch * 16, xrow, lb + SPX + k * 1024);
      }
    }
    WG_BARRIER();
    asm volatile("s_waitcnt vmcnt(0)" ::: "memory");
    STAMP(1);
    flag_arrive(lb + FLG + 20 + team * 4, lane);
    flag_wait(lb + FLG + 20 + team * 4, 4);
    const bool isA = ((w >> 1) ^ team) == 0;
    const int half = w & 1;
    const char* f0 = xrow + (size_t)(team * 7) * TOKB;
    const unsigned fo = 16 * (g ^ (3 * (i >> 3)));
    u32x4 Lf[6][3], Rf[3][3];
    f32x4 D[3][6];
    if (isA) {
      const unsigned lq = lb + W_OFF + (2 * team) * 18432, lk = lq + 18432;
#pragma unroll
      for (int rt = 0; rt < 6; ++rt)
#pragma unroll
        for (int s = 0; s < 3; ++s) Lf[rt][s] = lds_ld<u32x4>(lq + chan(rt, i) * TOKB + 64 * s + fo);
#pragma unroll
      for (int c = 0; c < 3; ++c)
#pragma unroll
        for (int s = 0; s < 3; ++s) Rf[c][s] = lds_ld<u32x4>(lk + chan(3 * half + c, i) * TOKB + 64 * s + fo);
      flag_arrive(lb + FLG + 28 + team * 4, lane);
      prep_products<true>(Lf, Rf, D, 0);
      flag_wait(lb + FLG + 28 + team * 4, 4);
      dma_group(dv[0], dv[1], dv[2], f0, f0 + FRAMEB, f0 + 2 * (size_t)FRAMEB, f0 + 3 * (size_t)FRAMEB, xb);
      prep_products<true>(Lf, Rf, D, 1);
      prep_products<true>(Lf, Rf, D, 2);
      prep_finish<true>(D, lb, team, half, lane);
    } else {
#pragma unroll
      for (int rt = 0; rt < 6; ++rt)
#pragma unroll
        for (int s = 0; s < 3; ++s) Lf[rt][s] = lds_ld<u32x4>(xt + chan(rt, i) * TOKB + 64 * s + fo);
#pragma unroll
      for (int c = 0; c < 3; ++c) {
        const int ct = 3 * half + c;
#pragma unroll
        for (int s = 0; s < 3; ++s) {
          const unsigned a = xt + 18432 + (32 * s + 8 * g + (i >> 2)) * 192 + 64 * (ct >> 1) + 16 * (i & 3) + 8 * (ct & 1);
          const u32x2 lo = lds_tr(a), hi = lds_tr(a + 4 * 192);
          Rf[c][s] = u32x4{lo[0], lo[1], hi[0], hi[1]};
        }
      }
      flag_arrive(lb + FLG + 28 + team * 4, lane);
      prep_products<false>(Lf, Rf, D, 0);
      flag_wait(lb + FLG + 28 + team * 4, 4);
      dma_group(dv[0], dv[1], dv[2], f0, f0 + FRAMEB, f0 + 2 * (size_t)FRAMEB, f0 + 3 * (size_t)FRAMEB, xb);
      prep_products<false>(Lf, Rf, D, 1);
      prep_products<false>(Lf, Rf, D, 2);
      prep_finish<false>(D, lb, team, half, lane);
    }
    STAMP(2);
    flag_arrive(lb + FLG + 44 + team * 4, lane);
    flag_wait(lb + FLG + 44, 4);
    if (wave == 4) flag_wait(lb + FLG + 48, 4);
    STAMP(3);
  }

  const int pq = i >> 2, tq = i & 3;
  const bool diag = (g == pq);
  const unsigned maskP = diag ? 0xffffffffu : 0u;
  const unsigned idv = diag ? (0x3F80u << (16 * (tq & 1))) : 0u;
  const unsigned ID0 = (tq & 2) ? 0u : idv, ID1 = (tq & 2) ? idv : 0u;
  const u32x4 SEL0 = {ID0, ID1, 0u, 0u}, SEL1 = {0u, 0u, ID0, ID1};
  const u32x4 ONES = {0x3F803F80u, 0x3F803F80u, 0x3F803F80u, 0x3F803F80u};
  const f32x4 Z4 = {0.f, 0.f, 0.f, 0.f};

  if (wave == 4) {
    constexpr int WMASK = 3;
    const unsigned wl = lb + W_OFF + lane * 16;
    u32x4 X[3];
#pragma unroll
    for (int s = 0; s < 3; ++s) X[s] = lds_ld<u32x4>(lb + SPX + i * TOKB + 64 * s + 16 * g);
    f32x4 G = Z4, Sm = Z4;
#pragma unroll
    for (int s = 0; s < 3; ++s) { G = mfma(X[s], X[s], G); Sm = mfma(ONES, X[s], Sm); }
    f32x4 acc[6];
#pragma unroll
    for (int ct = 0; ct < 6; ++ct) { acc[ct] = Z4;
#pragma unroll
      for (int s = 0; s < 3; ++s) acc[ct] = mfma(WFRAG(0, ct, s), X[s], acc[ct]); }
    f32x4 S = Z4;
#pragma unroll
    for (int s = 0; s < 3; ++s) S = mfma(X[s], pack8(acc[2 * s], acc[2 * s + 1]), S);
    const u32x2 P = softmax_t(S, rstd(sel4(G, tq), Sm[0]), maskP);
    u32x4 X1[3];
#pragma unroll
    for (int s = 0; s < 3; ++s) {
      f32x4 o[2];
#pragma unroll
      for (int hf = 0; hf < 2; ++hf) {
        f32x4 u = Z4;
#pragma unroll
        for (int k = 0; k < 3; ++k) u = mfma(X[k], WFRAG(1, 2 * s + hf, k), u);
        o[hf] = mfma(u32x4{pk(u[0], u[1]), pk(u[2], u[3]), 0u, 0u}, u32x4{P[0], P[1], 0u, 0u}, Z4);
        o[hf] = mfma(hf ? SEL1 : SEL0, X[s], o[hf]);
      }
      X1[s] = pack8(o[0], o[1]);
      lds_st<u32x4>(lb + SPX + i * TOKB + 64 * s + 16 * g, X1[s]);
    }
    G = Z4; Sm = Z4;
#pragma unroll
    for (int s = 0; s < 3; ++s) { G = mfma(X1[s], X1[s], G); Sm = mfma(ONES, X1[s], Sm); }
    const float r2 = rstd(sel4(G, tq), Sm[0]);
    if (diag) lds_st<float>(lb + SPR + i * 4, r2);
#pragma unroll
    for (int mm = 0; mm < 2; ++mm)
#pragma unroll
      for (int s = 0; s < 3; ++s) {
        f32x4 a0 = Z4, a1 = Z4;
#pragma unroll
        for (int k = 0; k < 3; ++k) { a0 = mfma(WFRAG(2 + mm, 2 * s, k), X1[k], a0); a1 = mfma(WFRAG(2 + mm, 2 * s + 1, k), X1[k], a1); }
        lds_st<u32x4>(lb + (mm ? SPU : SPG) + i * TOKB + 64 * s + 16 * g, pack8(a0, a1));
      }
    flag_arrive(lb + FLG + 16, lane);
  }

  STAMP(4);
  const __amdgpu_buffer_rsrc_t ors = __builtin_amdgcn_make_buffer_rsrc(out, 0, 4 * FRAMEB, 0x00020000);
  const unsigned obase = (unsigned)(wy * 7) * ROWB + (unsigned)(wx0 * 7) * TOKB + w * FRAMEB;
#define RA(j) (((j) & 1 ? raO : raE) + 768 * (j))
#define TA(j) (((j) & 1 ? taO : taE) + 768 * (j))

#pragma unroll 1
  for (int n = 0; n < 2; ++n) {
    const int wl4 = 2 * n + team;
    if (n == 0) asm volatile("s_waitcnt vmcnt(0)" ::: "memory");
    else        asm volatile("s_waitcnt vmcnt(9)" ::: "memory");
    STAMP(5);
    PRIO_S1();
    {
      int ln = lane; asm volatile("" : "+v"(ln));
      const int li = ln & 15, lg = ln >> 4, lp = li >> 2, lt = li & 3;
      const unsigned raE = xt + lt * FS + (12 * w + lp) * TOKB + 16 * (lg ^ swz(12 * w + lp)), raO = raE ^ 32;
      const unsigned taE = xt + lp * FS + (12 * w + lg) * TOKB + 16 * (lt ^ swz(12 * w + lg)), taO = taE ^ 32;
      const unsigned r2a = lb + R2_OFF + (team * 2 + n) * 1024 + lt * 256 + (lg == lp ? 12 * w + lp : 49) * 4;
      constexpr int WMASK = 1;
      const unsigned wl = lb + W_OFF + ln * 16;
      u32x4 X[3][3], Wc[3], Wn[3];
#pragma unroll
      for (int s = 0; s < 3; ++s) X[0][s] = lds_ld<u32x4>(RA(0) + 64 * s);
#pragma unroll
      for (int k = 0; k < 3; ++k) Wc[k] = WFRAG(0, 0, k);
#pragma unroll
      for (int j = 1; j < 3; ++j)
#pragma unroll
        for (int s = 0; s < 3; ++s) X[j][s] = lds_ld<u32x4>(RA(j) + 64 * s);
      SB();
      f32x4 G[3], Sm[3], acc[2][3];
      u32x4 Gp[3][3];
      float r1[3];
#pragma unroll
      for (int ct = 0; ct < 6; ++ct) {
#pragma unroll
        for (int k = 0; k < 3; ++k) Wn[k] = ct < 5 ? WFRAG(0, ct + 1, k) : WFRAG(1, 0, k);
        {
          const int j = ct >> 1;
          f32x4 a = Z4;
#pragma unroll
          for (int s = 0; s < 3; ++s) a = mfma((ct & 1) ? ONES : X[j][s], X[j][s], a);
          if (ct & 1) Sm[j] = a; else G[j] = a;
        }
#pragma unroll
        for (int k = 0; k < 3; ++k)
#pragma unroll
          for (int j = 0; j < 3; ++j) acc[ct & 1][j] = mfma(Wc[k], X[j][k], k ? acc[ct & 1][j] : Z4);
        if (ct > 0) {
#pragma unroll
          for (int j = 0; j < 3; ++j) {
            const f32x4 a = acc[(ct - 1) & 1][j];
            Gp[j][(ct - 1) >> 1][2 * ((ct - 1) & 1)] = pk(a[0], a[1]);
            Gp[j][(ct - 1) >> 1][2 * ((ct - 1) & 1) + 1] = pk(a[2], a[3]);
          }
        }
        if (ct == 2 || ct == 4) r1[(ct >> 1) - 1] = rstd(sel4(G[(ct >> 1) - 1], tq), Sm[(ct >> 1) - 1][0]);
        SB();
#pragma unroll
        for (int k = 0; k < 3; ++k) Wc[k] = Wn[k];
      }
      u32x2 Up[3][6], P[3];
      f32x4 S[3];
#pragma unroll
      for (int ct = 0; ct < 6; ++ct) {
        if (ct < 5) {
#pragma unroll
          for (int k = 0; k < 3; ++k) Wn[k] = WFRAG(1, ct + 1, k);
        }
#pragma unroll
        for (int k = 0; k < 3; ++k)
#pragma unroll
          for (int j = 0; j < 3; ++j) acc[ct & 1][j] = mfma(X[j][k], Wc[k], k ? acc[ct & 1][j] : Z4);
        if (ct == 0) {
#pragma unroll
          for (int j = 0; j < 3; ++j) { const f32x4 a = acc[1][j]; Gp[j][2][2] = pk(a[0], a[1]); Gp[j][2][3] = pk(a[2], a[3]); }
          r1[2] = rstd(sel4(G[2], tq), Sm[2][0]);
        } else {
#pragma unroll
          for (int j = 0; j < 3; ++j) { const f32x4 a = acc[(ct - 1) & 1][j]; Up[j][ct - 1] = u32x2{pk(a[0], a[1]), pk(a[2], a[3])}; }
        }
        if (ct == 1) {
#pragma unroll
          for (int s = 0; s < 3; ++s)
#pragma unroll
            for (int j = 0; j < 3; ++j) S[j] = mfma(X[j][s], Gp[j][s], s ? S[j] : Z4);
        }
        if (ct >= 2 && ct <= 4) P[ct - 2] = softmax_t(S[ct - 2], r1[ct - 2], maskP);
        SB();
#pragma unroll
        for (int k = 0; k < 3; ++k) Wc[k] = Wn[k];
      }
      u32x2 XT[2][3];
#pragma unroll
      for (int j = 0; j < 3; ++j) XT[0][j] = lds_tr(TA(j));
#pragma unroll
      for (int j = 0; j < 3; ++j) { const f32x4 a = acc[1][j]; Up[j][5] = u32x2{pk(a[0], a[1]), pk(a[2], a[3])}; }
      u32x4 X1[3][3];
      f32x4 G2[3], S2[3];
#pragma unroll
      for (int ct = 0; ct < 7; ++ct) {
        if (ct < 5) {
#pragma unroll
          for (int j = 0; j < 3; ++j) XT[(ct + 1) & 1][j] = lds_tr(TA(j) + 64 * ((ct + 1) >> 1) + 8 * ((ct + 1) & 1));
        }
        if (ct < 6) {
#pragma unroll
          for (int j = 0; j < 3; ++j)
            acc[ct & 1][j] = mfma(u32x4{Up[j][ct][0], Up[j][ct][1], XT[ct & 1][j][0], XT[ct & 1][j][1]}, u32x4{P[j][0], P[j][1], ID0, ID1}, Z4);
        }
        if (ct > 0) {
#pragma unroll
          for (int j = 0; j < 3; ++j) {
            const f32x4 a = acc[(ct - 1) & 1][j];
            X1[j][(ct - 1) >> 1][2 * ((ct - 1) & 1)] = pk(a[0], a[1]);
            X1[j][(ct - 1) >> 1][2 * ((ct - 1) & 1) + 1] = pk(a[2], a[3]);
          }
        }
        if (ct >= 3 && (ct & 1)) {
          const int s = (ct - 3) >> 1;
#pragma unroll
          for (int j = 0; j < 3; ++j) { G2[j] = mfma(X1[j][s], X1[j][s], s ? G2[j] : Z4); S2[j] = mfma(ONES, X1[j][s], s ? S2[j] : Z4); }
        }
        SB();
      }
#pragma unroll
      for (int j = 0; j < 3; ++j) { G2[j] = mfma(X1[j][2], X1[j][2], G2[j]); S2[j] = mfma(ONES, X1[j][2], S2[j]); }
#pragma unroll
      for (int j = 0; j < 3; ++j)
#pragma unroll
        for (int s = 0; s < 3; ++s) lds_st<u32x4>(RA(j) + 64 * s, X1[j][s]);
#pragma unroll
      for (int j = 0; j < 3; ++j) lds_st<float>(r2a + 16 * j, rstd(sel4(G2[j], tq), S2[j][0]));
    }
    STAMP(6);
    flag_arrive(lb + FLG + team * 8, lane);
    if (n == 0) flag_wait(lb + FLG + 16, 1);
    flag_wait(lb + FLG + team * 8, 4 * (n + 1));
    STAMP(7);
    PRIO_S2();

    {
      int ln = lane; asm volatile("" : "+v"(ln));
      const int li = ln & 15, lg = ln >> 4;
      const int tok = 4 * wl4 + w;
      const unsigned rb2 = xt + w * FS + li * TOKB + 16 * (lg ^ swz(li));
      const unsigned r2f = lb + R2_OFF + (team * 2 + n) * 1024 + w * 256;
      const unsigned spa = lb + SPX + tok * TOKB + 16 * lg;
      const unsigned sua = lb + SPU + tok * TOKB + 2 * chan(0, li);
      const unsigned psc = lb + PSC + wave * 128;
      const unsigned osoff = obase + wl4 * 7 * TOKB;
      constexpr int WMASK = 1;
      const unsigned wl = lb + W_OFF + 36864 + ln * 16;
      u32x4 X1[3][3], XS[3], GS[3], Wc[3], Wn[3];
#pragma unroll
      for (int s = 0; s < 3; ++s) X1[0][s] = lds_ld<u32x4>(rb2 + 64 * s);
#pragma unroll
      for (int k = 0; k < 3; ++k) Wc[k] = WFRAG(3, 0, k);
#pragma unroll
      for (int t = 1; t < 3; ++t)
#pragma unroll
        for (int s = 0; s < 3; ++s) X1[t][s] = lds_ld<u32x4>(rb2 + t * 3072 + 64 * s);
      flag_arrive(lb + FLG + team * 8 + 4, lane);
      const float rs = lds_ld<float>(lb + SPR + tok * 4);
      const float rd = lds_ld<float>(lg < 3 ? r2f + ln * 4 : lb + SPR + tok * 4);
#pragma unroll
      for (int s = 0; s < 3; ++s) { GS[s] = lds_ld<u32x4>(spa + (SPG - SPX) + 64 * s); XS[s] = lds_ld<u32x4>(spa + 64 * s); }
      SB();
      STAMP(8);

      u32x4 UA[6][2];
      f32x4 acc[2][3], Sd[4];
      float de, dt;
      u32x4 PS0, PS1;
#pragma unroll
      for (int ct = 0; ct < 7; ++ct) {
        unsigned us;
        if (ct < 5) {
#pragma unroll
          for (int k = 0; k < 3; ++k) Wn[k] = WFRAG(3, ct + 1, k);
        }
        if (ct > 0) us = lds_ld<unsigned short>(sua + 64 * ((ct - 1) >> 1) + 8 * ((ct - 1) & 1));
        if (ct == 6) {
          const u32x2 p0 = lds_ld<u32x2>(psc + lg * 8), p1 = lds_ld<u32x2>(psc + 32 + lg * 8), p2 = lds_ld<u32x2>(psc + 64 + lg * 8), p3 = lds_ld<u32x2>(psc + 96 + lg * 8);
          PS0 = u32x4{p0[0], p0[1], p1[0], p1[1]}; PS1 = u32x4{p2[0], p2[1], p3[0], p3[1]};
        }
        if (ct < 6) {
#pragma unroll
          for (int k = 0; k < 3; ++k)
#pragma unroll
            for (int t = 0; t < 3; ++t) acc[ct & 1][t] = mfma(X1[t][k], Wc[k], k ? acc[ct & 1][t] : Z4);
        }
        if (ct < 2) {
#pragma unroll
          for (int kt = 2 * ct; kt < 2 * ct + 2; ++kt)
#pragma unroll
            for (int s = 0; s < 3; ++s) Sd[kt] = mfma(GS[s], kt < 3 ? X1[kt < 3 ? kt : 0][s] : XS[s], s ? Sd[kt] : Z4);
        }
        if (ct > 0) {
          const f32x4 a = acc[(ct - 1) & 1][0], b = acc[(ct - 1) & 1][1], c = acc[(ct - 1) & 1][2];
          UA[ct - 1][0] = u32x4{pk(a[0], a[1]), pk(a[2], a[3]), pk(b[0], b[1]), pk(b[2], b[3])};
          UA[ct - 1][1] = u32x4{pk(c[0], c[1]), pk(c[2], c[3]), us, 0u};
        }
        if (ct == 2) {
          const float d = lg == 0 ? Sd[0][0] : lg == 1 ? Sd[1][0] : lg == 2 ? Sd[2][0] : Sd[3][0];
          dt = fmaf(d, rd, (lg == 3 && li != 0) ? NEG_BIG : 0.f);
          de = inrow_max(dt);
        }
        if (ct == 3) {
          const float c = rs * KS;
          de = exp2_(fmaf(dt, c, -xrow_max(de) * c));
          dt = inrow_sum(de);
        }
        if (ct == 4) {
          const float inv = __builtin_amdgcn_rcpf(xrow_sum(dt));
          lds_st<unsigned short>(psc + ln * 2, (unsigned short)pk(de * rd * inv, 0.f));
        }
        SB();
        if (ct < 5) {
#pragma unroll
          for (int k = 0; k < 3; ++k) Wc[k] = Wn[k];
        }
      }
      STAMP(9);
#pragma unroll
      for (int k = 0; k < 3; ++k) Wc[k] = WFRAG(2, 0, k);
      {
        const unsigned voff = li == 0 ? osoff + 6 * ROWB + 6 * TOKB + 16 * lg : 0x7ffffff0u;
#pragma unroll
        for (int s = 0; s < 3; ++s) {
          f32x4 o0 = mfma(UA[2 * s][0], PS0, Z4), o1 = mfma(UA[2 * s + 1][0], PS0, Z4);
          o0 = mfma(UA[2 * s][1], PS1, o0); o1 = mfma(UA[2 * s + 1][1], PS1, o1);
          o0 = mfma(SEL0, XS[s], o0); o1 = mfma(SEL1, XS[s], o1);
          bstore(ors, pack8(o0, o1), voff + 64 * s);
        }
      }
      if (n == 0) {
        flag_wait(lb + FLG + team * 8 + 4, 4);
        const char* f0 = xrow + (size_t)((2 + team) * 7) * TOKB;
        dma_group(dv[0], dv[1], dv[2], f0, f0 + FRAMEB, f0 + 2 * (size_t)FRAMEB, f0 + 3 * (size_t)FRAMEB, xb);
      }
      SB();
      STAMP(10);
      u32x4 Gp[3][3];
      f32x4 Rr[3]; float rc[3];
#pragma unroll
      for (int ct = 0; ct < 7; ++ct) {
        if (ct < 5) {
#pragma unroll
          for (int k = 0; k < 3; ++k) Wn[k] = WFRAG(2, ct + 1, k);
        }
        if (ct == 5) {
#pragma unroll
          for (int t = 0; t < 3; ++t) { Rr[t] = lds_ld<f32x4>(r2f + t * 64 + lg * 16); rc[t] = lds_ld<float>(r2f + t * 64 + li * 4); }
#pragma unroll
          for (int s = 0; s < 3; ++s) XS[s] = lds_ld<u32x4>(spa + 64 * s);
        }
        if (ct < 6) {
#pragma unroll
          for (int k = 0; k < 3; ++k)
#pragma unroll
            for (int t = 0; t < 3; ++t) acc[ct & 1][t] = mfma(Wc[k], X1[t][k], k ? acc[ct & 1][t] : Z4);
        }
        if (ct > 0) {
#pragma unroll
          for (int t = 0; t < 3; ++t) {
            const f32x4 a = acc[(ct - 1) & 1][t];
            Gp[t][(ct - 1) >> 1][2 * ((ct - 1) & 1)] = pk(a[0], a[1]);
            Gp[t][(ct - 1) >> 1][2 * ((ct - 1) & 1) + 1] = pk(a[2], a[3]);
          }
        }
        SB();
        if (ct < 5) {
#pragma unroll
          for (int k = 0; k < 3; ++k) Wc[k] = Wn[k];
        }
      }
      STAMP(11);
      f32x4 S[2][3], Sx[2];
      u32x4 P0, P1;
      const float biask = lg == 0 ? 0.f : NEG_BIG;
      auto scores = [&](int t, int b) __attribute__((always_inline)) {
#pragma unroll
        for (int s = 0; s < 3; ++s) {
#pragma unroll
          for (int kt = 0; kt < 3; ++kt) S[b][kt] = mfma(X1[kt][s], Gp[t][s], s ? S[b][kt] : Z4);
          Sx[b] = mfma(XS[s], Gp[t][s], s ? Sx[b] : Z4);
        }
      };
      auto pv = [&](int t, const u32x4& p0, const u32x4& p1) __attribute__((always_inline)) {
        const int pos = 16 * t + li, y = (pos * 37) >> 8;
        const unsigned voff = osoff + pos * TOKB + y * (ROWB - 7 * TOKB) + 16 * lg;
#pragma unroll
        for (int s = 0; s < 3; ++s) {
          f32x4 o0 = mfma(UA[2 * s][0], p0, Z4), o1 = mfma(UA[2 * s + 1][0], p0, Z4);
          o0 = mfma(UA[2 * s][1], p1, o0); o1 = mfma(UA[2 * s + 1][1], p1, o1);
          o0 = mfma(SEL0, X1[t][s], o0); o1 = mfma(SEL1, X1[t][s], o1);
          bstore(ors, pack8(o0, o1), voff + 64 * s);
        }
      };
      scores(0, 0);
      SB();
#pragma unroll
      for (int t = 0; t < 3; ++t) {
        const int b = t & 1;
        if (t < 2) scores(t + 1, b ^ 1);
        u32x4 Q0 = P0, Q1 = P1;
        {
          f32x4 e[3];
#pragma unroll
          for (int kt = 0; kt < 3; ++kt)
#pragma unroll
            for (int k = 0; k < 4; ++k) e[kt][k] = S[b][kt][k] * Rr[kt][k];
          float tx = fmaf(Sx[b][0], rs, biask);
          float mx = max3(max3(e[0][0], e[0][1], e[0][2]), max3(e[0][3], e[1][0], e[1][1]), max3(e[1][2], e[1][3], e[2][0]));
          mx = max3(mx, max3(e[2][1], e[2][2], e[2][3]), tx);
          mx = xrow_max(mx);
          const float c = rc[t] * KS, mn = -mx * c;
#pragma unroll
          for (int kt = 0; kt < 3; ++kt)
#pragma unroll
            for (int k = 0; k < 4; ++k) e[kt][k] = exp2_(fmaf(e[kt][k], c, mn));
          tx = exp2_(fmaf(tx, c, mn));
          float l = ((e[0][0] + e[0][1]) + (e[0][2] + e[0][3])) + ((e[1][0] + e[1][1]) + (e[1][2] + e[1][3]));
          l += ((e[2][0] + e[2][1]) + (e[2][2] + e[2][3])) + tx;
          const float inv = __builtin_amdgcn_rcpf(xrow_sum(l));
#pragma unroll
          for (int kt = 0; kt < 3; ++kt)
#pragma unroll
            for (int k = 0; k < 4; ++k) e[kt][k] = e[kt][k] * Rr[kt][k] * inv;
          tx = tx * rs * inv;
          P0 = pack8(e[0], e[1]);
          P1 = u32x4{pk(e[2][0], e[2][1]), pk(e[2][2], e[2][3]), pk(tx, 0.f), 0u};
        }
        if (t > 0) pv(t - 1, Q0, Q1);
        ATT_SCHED(t);
        SB();
      }
      STAMP(12);
      pv(2, P0, P1);
      STAMP(13);
    }
  }
}

}

extern "C" void kernel_launch(void* const* d_in, const int* in_sizes, int n_in, void* d_out, int out_size,
                              void* d_ws, size_t ws_size, hipStream_t stream) {
  vs::swin_kernel<<<256, 512, 0, stream>>>((const char*)d_in[0], (const char*)d_in[1], (const char*)d_in[2],
                                           (const char*)d_in[3], (const char*)d_in[4], (char*)d_out, (char*)d_ws);
}
```

```cpp
#include <hip/hip_runtime.h>
#include <hip/hip_bf16.h>

namespace vs {

typedef __bf16 bf16x8 __attribute__((ext_vector_type(8)));
typedef __bf16 bf16x2 __attribute__((ext_vector_type(2)));
typedef short s16x4 __attribute__((ext_vector_type(4)));
typedef float f32x4 __attribute__((ext_vector_type(4)));
typedef float f32x2 __attribute__((ext_vector_type(2)));
typedef unsigned u32x4 __attribute__((ext_vector_type(4)));
typedef unsigned u32x2 __attribute__((ext_vector_type(2)));
#define AS3 __attribute__((address_space(3)))
#define DI __device__ __forceinline__
#if defined(__HIP_DEVICE_COMPILE__)
#define NOPK __attribute__((target("no-packed-fp32-ops")))
#else
#define NOPK
#endif
#define SB() __builtin_amdgcn_sched_barrier(0)
#define PRIO_PAT 0x1f
#define PRIO_A(k) asm volatile("s_cmp_lg_u32 %0, 0\n\ts_cbranch_scc1 9f\n\ts_cmp_eq_u32 %1, 0\n\ts_cbranch_scc1 1f\n\ts_setprio %3\n\ts_branch 9f\n1:\n\ts_setprio %2\n9:" \
    :: "s"(team), "s"(n), "i"((PRIO_PAT >> (k)) & 1), "i"((PRIO_PAT >> ((k) + 3)) & 1) : "scc")
#define PRIO_S1() PRIO_A(0)
#define PRIO_S2() PRIO_A(1)
#define PRIO_SET(v) PRIO_A(2)
#define KEEP(x) asm volatile("" :: "v"(x))
#define WG_BARRIER() do { asm volatile("s_waitcnt lgkmcnt(0)" ::: "memory"); __builtin_amdgcn_s_barrier(); asm volatile("" ::: "memory"); } while (0)
DI float u2f(unsigned u) { return __builtin_bit_cast(float, u); }

constexpr int TOKB = 192;
constexpr int ROWB = 224 * TOKB;
constexpr int FRAMEB = 224 * ROWB;
constexpr int FS = 9216 + 64;
constexpr int XTEAM = 4 * FS;
constexpr int W_OFF = 0;
constexpr int X_OFF = 73728;
constexpr int SPX = X_OFF + 2 * XTEAM;
constexpr int SPG = SPX + 3072;
constexpr int SPU = SPG + 3072;
constexpr int R2_OFF = SPU + 3072;
constexpr int SPR = R2_OFF + 4096;
constexpr int PSC = SPR + 64;
constexpr int FLG = PSC + 1024;
constexpr int DUMMY = FLG + 64;
constexpr int LDS_BYTES = DUMMY + 256;

constexpr float INV_C = 1.0f / 96.0f;
constexpr float EPS = 1e-5f;
constexpr float KS = 0.10206207261596577f * 1.4426950408889634f;
constexpr float NEG_BIG = -3.0e38f;
constexpr float EPS96 = 9216.0f * EPS;
constexpr float SCALE_T = KS * 9216.0f;
constexpr float SCALE_S = KS;

DI f32x4 mfma(u32x4 a, u32x4 b, f32x4 c) {
  return __builtin_amdgcn_mfma_f32_16x16x32_bf16(__builtin_bit_cast(bf16x8, a), __builtin_bit_cast(bf16x8, b), c, 0, 0, 0);
}
DI unsigned pk(float a, float b) { f32x2 v = {a, b}; return __builtin_bit_cast(unsigned, __builtin_convertvector(v, bf16x2)); }
template <class T> DI T lds_ld(unsigned a) { return *(AS3 T*)a; }
template <class T> DI void lds_st(unsigned a, T v) { *(AS3 T*)a = v; }
DI u32x2 lds_tr(unsigned a) { return __builtin_bit_cast(u32x2, __builtin_amdgcn_ds_read_tr16_b64_v4i16((AS3 s16x4*)a)); }
template <int CTRL> DI float dpp(float v) { return __builtin_bit_cast(float, __builtin_amdgcn_update_dpp(0, __builtin_bit_cast(int, v), CTRL, 0xf, 0xf, true)); }
DI float max2(float a, float b) { return __builtin_elementwise_maximum(a, b); }
DI float max3(float a, float b, float c) { return __builtin_elementwise_maximum(__builtin_elementwise_maximum(a, b), c); }
DI float exp2_(float x) { return __builtin_amdgcn_exp2f(x); }
DI void flag_arrive(unsigned addr, int lane) {
  unsigned a = addr, one = 1;
  asm volatile("s_mov_b64 exec, 1\n\tds_add_u32 %0, %1\n\ts_mov_b64 exec, -1" :: "v"(a), "v"(one) : "memory");
}
DI float opaque(float x) { asm volatile("" : "+v"(x)); return x; }
DI unsigned opaque(unsigned x) { asm volatile("" : "+v"(x)); return x; }
DI unsigned vcopy(unsigned x) { asm volatile("" : "+v"(x)); return x; }
DI unsigned f2u(float f) { return __builtin_bit_cast(unsigned, f); }
DI void flag_wait2(unsigned a0, unsigned t0, unsigned a1, unsigned t1) {
  unsigned v0, v1, s0, s1, cnt;
  asm volatile(
      "s_movk_i32 %4, 0x7fff\n"
      "1:\n\t"
      "ds_read_b32 %0, %5\n\t"
      "ds_read_b32 %1, %7\n\t"
      "s_waitcnt lgkmcnt(0)\n\t"
      "v_readfirstlane_b32 %2, %0\n\t"
      "v_readfirstlane_b32 %3, %1\n\t"
      "s_nop 1\n\t"
      "s_cmp_ge_u32 %2, %6\n\t"
      "s_cselect_b32 %3, %3, 0\n\t"
      "s_cmp_ge_u32 %3, %8\n\t"
      "s_cbranch_scc1 2f\n\t"
      "s_sleep 1\n\t"
      "s_sub_u32 %4, %4, 1\n\t"
      "s_cmp_lg_u32 %4, 0\n\t"
      "s_cbranch_scc1 1b\n"
      "2:"
      : "=&v"(v0), "=&v"(v1), "=&s"(s0), "=&s"(s1), "=&s"(cnt) : "v"(a0), "s"(t0), "v"(a1), "s"(t1) : "memory", "scc");
}
DI void flag_wait_seen(unsigned seen, unsigned addr, unsigned target, unsigned skip) {
  unsigned v, s, cnt;
  asm volatile(
      "s_cmp_lg_u32 %6, 0\n\t"
      "s_cbranch_scc1 2f\n\t"
      "v_readfirstlane_b32 %1, %3\n\t"
      "s_movk_i32 %2, 0x7fff\n\t"
      "s_nop 0\n\t"
      "s_cmp_ge_u32 %1, %5\n\t"
      "s_cbranch_scc1 2f\n"
      "1:\n\t"
      "ds_read_b32 %0, %4\n\t"
      "s_waitcnt lgkmcnt(0)\n\t"
      "v_readfirstlane_b32 %1, %0\n\t"
      "s_nop 1\n\t"
      "s_cmp_ge_u32 %1, %5\n\t"
      "s_cbranch_scc1 2f\n\t"
      "s_sleep 1\n\t"
      "s_sub_u32 %2, %2, 1\n\t"
      "s_cmp_lg_u32 %2, 0\n\t"
      "s_cbranch_scc1 1b\n"
      "2:"
      : "=&v"(v), "=&s"(s), "=&s"(cnt) : "v"(seen), "v"(addr), "s"(target), "s"(skip) : "memory", "scc");
}
DI void flag_wait(unsigned addr, unsigned target) {
  unsigned v, s, cnt;
  asm volatile(
      "s_movk_i32 %2, 0x7fff\n"
      "1:\n\t"
      "ds_read_b32 %0, %3\n\t"
      "s_waitcnt lgkmcnt(0)\n\t"
      "v_readfirstlane_b32 %1, %0\n\t"
      "s_nop 1\n\t"
      "s_cmp_ge_u32 %1, %4\n\t"
      "s_cbranch_scc1 2f\n\t"
      "s_sleep 1\n\t"
      "s_sub_u32 %2, %2, 1\n\t"
      "s_cmp_lg_u32 %2, 0\n\t"
      "s_cbranch_scc1 1b\n"
      "2:"
      : "=&v"(v), "=&s"(s), "=&s"(cnt) : "v"(addr), "s"(target) : "memory", "scc");
}

constexpr int STORES_AFTER_DMA = 12;
#define NT_LOAD " nt"
template <int J> DI void dma_tile(unsigned v, const char* f0, const char* f1, const char* f2, const char* f3, unsigned xb, unsigned skip) {
  unsigned keep;
#define D1(F, OFF) "s_add_u32 m0, %6, " OFF "\n\ts_nop 0\n\tglobal_load_lds_dwordx4 %1, " F NT_LOAD "\n\t"
  asm volatile(
      "s_cmp_lg_u32 %8, 0\n\t"
      "s_cbranch_scc1 1f\n\t"
      "s_mov_b32 %0, m0\n\t"
      "s_mov_b32 exec_hi, 0xffff\n\t"
      D1("%2", "%7") D1("%3", "%7+9280") D1("%4", "%7+18560") D1("%5", "%7+27840")
      "s_mov_b32 exec_hi, -1\n\t"
      "s_mov_b32 m0, %0\n"
      "1:"
      : "=&s"(keep)
      : "v"(v), "s"(f0), "s"(f1), "s"(f2), "s"(f3), "s"(xb), "i"(768 * J), "s"(skip)
      : "memory", "scc");
#undef D1
}
DI void dma_1k(unsigned voff, const char* base, unsigned ldsaddr) {
  unsigned keep;
  asm volatile("s_mov_b32 %0, m0\n\ts_mov_b32 m0, %3\n\ts_nop 0\n\tglobal_load_lds_dwordx4 %1, %2\n\ts_mov_b32 m0, %0"
               : "=&s"(keep) : "v"(voff), "s"(base), "s"(ldsaddr) : "memory");
}
#define ST_AUX 17
DI void bstore(__amdgpu_buffer_rsrc_t rs, u32x4 v, unsigned voff) { __builtin_amdgcn_raw_buffer_store_b128(v, rs, voff, 0, ST_AUX); }
DI int chan(int ct, int i) { return 32 * (ct >> 1) + 8 * (i >> 2) + 4 * (ct & 1) + (i & 3); }
DI unsigned swz(int pos) { return 2u * ((pos ^ (pos >> 2)) & 1); }

#define WFRAG(m, ct, s) lds_ld<u32x4>(wl + ((((m) & WMASK) * 6 + (ct)) * 3 + (s)) * 1024)

DI void dma_matrix(const unsigned (&ve)[3], const unsigned (&vo)[3], const char* src, unsigned blkstride, unsigned dst) {
  unsigned keep;
  const char* s1 = src + blkstride; const char* s2 = s1 + blkstride; const char* s3 = s2 + blkstride;
  const char* s4 = s3 + blkstride; const char* s5 = s4 + blkstride;
#define D1(V, F, OFF) "s_add_u32 m0, %13, " #OFF "\n\ts_nop 0\n\tglobal_load_lds_dwordx4 " V ", " F "\n\t"
  asm volatile(
      "s_mov_b32 %0, m0\n\t"
      D1("%1", "%7", 0) D1("%2", "%7", 1024) D1("%3", "%7", 2048)
      D1("%4", "%8", 3072) D1("%5", "%8", 4096) D1("%6", "%8", 5120)
      D1("%1", "%9", 6144) D1("%2", "%9", 7168) D1("%3", "%9", 8192)
      D1("%4", "%10", 9216) D1("%5", "%10", 10240) D1("%6", "%10", 11264)
      D1("%1", "%11", 12288) D1("%2", "%11", 13312) D1("%3", "%11", 14336)
      D1("%4", "%12", 15360) D1("%5", "%12", 16384) D1("%6", "%12", 17408)
      "s_mov_b32 m0, %0"
      : "=&s"(keep)
      : "v"(ve[0]), "v"(ve[1]), "v"(ve[2]), "v"(vo[0]), "v"(vo[1]), "v"(vo[2]),
        "s"(src), "s"(s1), "s"(s2), "s"(s3), "s"(s4), "s"(s5), "s"(dst)
      : "memory", "scc");
#undef D1
}

NOPK __global__ void __launch_bounds__(512, 2)
swin_kernel(const char* __restrict__ x, const char* __restrict__ wqkv_t, const char* __restrict__ wo_t,
            const char* __restrict__ wqkv_s, const char* __restrict__ wo_s, char* __restrict__ out) {
  __shared__ __attribute__((aligned(256))) char lds_raw[LDS_BYTES];
  const unsigned lb = (unsigned)(size_t)(AS3 char*)lds_raw;
  const int lane = threadIdx.x & 63;
  const int wave = __builtin_amdgcn_readfirstlane(threadIdx.x >> 6);
  const int team = wave >> 2, w = wave & 3;
  const int i = lane & 15, g = lane >> 4;
  const int wy = blockIdx.x >> 3, wx0 = (blockIdx.x & 7) * 4;
  const char* xrow = x + (size_t)(wy * 7) * ROWB + (size_t)(wx0 * 7) * TOKB;
  const unsigned xt = lb + X_OFF + team * XTEAM;

  unsigned dv[3];
  const unsigned xb = xt + 12 * w * TOKB;
  __builtin_amdgcn_s_setprio(1);
  {
    const char* wq = team ? wqkv_s : wqkv_t;
    const char* wo = team ? wo_s : wo_t;
    if (threadIdx.x < 16) lds_st<unsigned>(lb + FLG + threadIdx.x * 4, 0u);
    unsigned ve[3], vo[3];
    const unsigned rstride = w < 3 ? 576 : 192;
#pragma unroll
    for (int k = 0; k < 3; ++k) {
      const int nn = 64 * k + lane, row = nn / 12, ch = nn % 12;
      ve[k] = row * rstride + ch * 16;
      vo[k] = row * rstride + (w < 3 ? ch ^ 3 : ch) * 16;
    }
    const char* src = w < 3 ? wq + w * 192 : wo;
    const unsigned xs = xt;
    const unsigned dst = w < 2 ? lb + W_OFF + (2 * team + w) * 18432 : xs + (w - 2) * 18432;
    const char* f0 = xrow + (size_t)(team * 7) * TOKB;
    if (team == 0) dma_matrix(ve, vo, src, 16 * rstride, dst);
    if (wave == 0) {
      unsigned long long pc; unsigned len, keep;
      asm volatile(".Lvs_begin:\n\ts_getpc_b64 %0\n\ts_mov_b32 %1, .Lvs_end-.Lvs_begin" : "=s"(pc), "=s"(len));
      unsigned off = (((blockIdx.x >> 3) & 31) + 32 * lane) * 64;
      off = off < len - 64 ? off : len - 64;
      asm volatile("s_mov_b32 %0, m0\n\ts_mov_b32 m0, %3\n\ts_nop 0\n\tglobal_load_lds_dword %1, %2\n\ts_mov_b32 m0, %0"
                   : "=&s"(keep) : "v"(off), "s"(pc), "s"(lb + DUMMY) : "memory");
    }
    WG_BARRIER();
    if (team == 1) {
      dma_matrix(ve, vo, src, 16 * rstride, dst);
    } else if (wave == 0) {
#pragma unroll
      for (int k = 0; k < 3; ++k) {
        const int nn = 64 * k + lane, tok = nn / 12, ch = nn % 12;
        dma_1k((tok & 3) * FRAMEB + 6 * ROWB + ((tok >> 2) * 7 + 6) * TOKB + ch * 16, xrow, lb + SPX + k * 1024);
      }
    }
    {
      const int l48 = lane < 48 ? lane : 47;
      const int pp = l48 / 12, ch = l48 % 12;
  #pragma unroll
      for (int j = 0; j < 3; ++j) {
        const int pos = 12 * w + 4 * j + pp;
        dv[j] = ((pos / 7) * 224 + pos % 7) * TOKB + (ch ^ swz(pos)) * 16;
      }
    }
    const bool isA = ((w >> 1) ^ team) == 0;
    const int half = w & 1;
    const unsigned lq = lb + W_OFF + (2 * team) * 18432, lk = lq + 18432;
    const unsigned mu = lb + SPG + team * 576;
    const unsigned bl = (8 * g + (i >> 2)) * TOKB + 16 * (((i & 3) >> 1) ^ (3 * (g >> 1))) + 8 * (i & 1);
    unsigned cav[5], mav[5];
#pragma unroll
    for (int k = 0; k < 5; ++k) {
      const int tt = w + 4 * k < 18 ? w + 4 * k : 17;
      const int m = tt / 6, dt = tt - 6 * m;
      cav[k] = (m < 2 ? lq + m * 18432 : xs) + 64 * (dt >> 1) + (bl ^ (32 * (dt & 1)));
      mav[k] = mu + m * 192 + (16 * dt + i) * 2;
    }
    const unsigned fo = (8 * (i >> 2) + (i & 3)) * TOKB + 16 * (g ^ (3 * (i >> 3)));
    const f32x4 Z4 = {0.f, 0.f, 0.f, 0.f};
    const u32x4 ONES = {0x3F803F80u, 0x3F803F80u, 0x3F803F80u, 0x3F803F80u};
    const unsigned fP2 = lb + FLG + 28 + team * 4;
#define XDMA(j) dma_tile<j>(dv[j], f0, f0 + FRAMEB, f0 + 2 * (size_t)FRAMEB, f0 + 3 * (size_t)FRAMEB, xb, 0u)
    unsigned la = (isA ? lq : xs) + fo;
    unsigned ra0 = isA ? lk + fo + (half ? 36 * TOKB : 0) : xs + 18432 + (8 * g + (i >> 2)) * 192 + 16 * (i & 3) + (half ? 72 : 0);
    unsigned ra1 = ra0 + (isA ? (half ? 28 : 4) * TOKB : (half ? 56 : 8)), ra2 = ra0 + (isA ? 32 * TOKB : 64);
    unsigned ca0 = cav[0], ca1 = cav[1], ca2 = cav[2], ca3 = cav[3], ca4 = cav[4];
    unsigned ma0 = mav[0], ma1 = mav[1], ma2 = mav[2], ma3 = mav[3], ma4 = mav[4];
    unsigned wdst = lb + W_OFF + ((2 * team + (isA ? 0 : 1)) * 6 + 3 * half) * 3072 + lane * 16, mub = mu + 16 * g;
    asm volatile("" : "+v"(la), "+v"(ra0), "+v"(ra1), "+v"(ra2), "+v"(ca0), "+v"(ca1), "+v"(ca2), "+v"(ca3), "+v"(ca4),
                      "+v"(ma0), "+v"(ma1), "+v"(ma2), "+v"(ma3), "+v"(ma4), "+v"(wdst), "+v"(mub));
    if (wave == 0) asm volatile("s_waitcnt vmcnt(3)" ::: "memory");
    else asm volatile("s_waitcnt vmcnt(0)" ::: "memory");
    flag_arrive(lb + FLG + 20 + team * 4, lane);
    flag_wait(lb + FLG + 20 + team * 4, 4);
#define flag_arrive(a) flag_arrive(a, lane)
    if (isA) {
      const float scale = team ? SCALE_S : SCALE_T;
      SB();
      const u32x2 cl00_r = lds_tr(ca0 + 0); SB();
      const u32x2 ch00_r = lds_tr(ca0 + 768); SB();
      const u32x2 cl01_r = lds_tr(ca0 + 6144); SB();
      const u32x2 ch01_r = lds_tr(ca0 + 6912); SB();
      const u32x2 cl02_r = lds_tr(ca0 + 12288); SB();
      const u32x2 ch02_r = lds_tr(ca0 + 13056); SB();
      const u32x2 cl10_r = lds_tr(ca1 + 0); SB();
      const u32x2 ch10_r = lds_tr(ca1 + 768); SB();
      const u32x2 cl11_r = lds_tr(ca1 + 6144); SB();
      const u32x2 ch11_r = lds_tr(ca1 + 6912); SB();
      const u32x2 cl12_r = lds_tr(ca1 + 12288); SB();
      const u32x2 ch12_r = lds_tr(ca1 + 13056); SB();
      const u32x2 cl20_r = lds_tr(ca2 + 0); SB();
      const u32x2 ch20_r = lds_tr(ca2 + 768); SB();
      const u32x2 cl21_r = lds_tr(ca2 + 6144); SB();
      const u32x2 ch21_r = lds_tr(ca2 + 6912); SB();
      const u32x2 cl22_r = lds_tr(ca2 + 12288); SB();
      const u32x2 ch22_r = lds_tr(ca2 + 13056); SB();
      const u32x2 cl30_r = lds_tr(ca3 + 0); SB();
      const u32x2 ch30_r = lds_tr(ca3 + 768); SB();
      const u32x2 cl31_r = lds_tr(ca3 + 6144); SB();
      const u32x2 ch31_r = lds_tr(ca3 + 6912); SB();
      const u32x2 cl32_r = lds_tr(ca3 + 12288); SB();
      const u32x2 ch32_r = lds_tr(ca3 + 13056); SB();
      const u32x2 cl40_r = lds_tr(ca4 + 0); SB();
      const u32x2 ch40_r = lds_tr(ca4 + 768); SB();
      const u32x2 cl41_r = lds_tr(ca4 + 6144); SB();
      const u32x2 ch41_r = lds_tr(ca4 + 6912); SB();
      const u32x2 cl42_r = lds_tr(ca4 + 12288); SB();
      const u32x2 ch42_r = lds_tr(ca4 + 13056); SB();
      const u32x4 L00_r = lds_ld<u32x4>(la + 0); SB();
      const u32x4 L01_r = lds_ld<u32x4>(la + 64); SB();
      const u32x4 L02_r = lds_ld<u32x4>(la + 128); SB();
      const u32x4 R00_r = lds_ld<u32x4>(ra0 + 0); SB();
      const u32x4 R01_r = lds_ld<u32x4>(ra0 + 64); SB();
      const u32x4 R02_r = lds_ld<u32x4>(ra0 + 128); SB();
      const u32x4 L10_r = lds_ld<u32x4>(la + 768); SB();
      const u32x4 L11_r = lds_ld<u32x4>(la + 832); SB();
      const u32x4 L12_r = lds_ld<u32x4>(la + 896); SB();
      const u32x4 L20_r = lds_ld<u32x4>(la + 6144); SB();
      const u32x4 L21_r = lds_ld<u32x4>(la + 6208); SB();
      const u32x4 L22_r = lds_ld<u32x4>(la + 6272); SB();
      const u32x4 L30_r = lds_ld<u32x4>(la + 6912); SB();
      const u32x4 L31_r = lds_ld<u32x4>(la + 6976); SB();
      const u32x4 L32_r = lds_ld<u32x4>(la + 7040); SB();
      const u32x4 L40_r = lds_ld<u32x4>(la + 12288); SB();
      const u32x4 L41_r = lds_ld<u32x4>(la + 12352); SB();
      const u32x4 L42_r = lds_ld<u32x4>(la + 12416); SB();
      const u32x4 L50_r = lds_ld<u32x4>(la + 13056); SB();
      const u32x4 L51_r = lds_ld<u32x4>(la + 13120); SB();
      const u32x4 L52_r = lds_ld<u32x4>(la + 13184); SB();
      const u32x4 R10_r = lds_ld<u32x4>(ra1 + 0); SB();
      const u32x4 R11_r = lds_ld<u32x4>(ra1 + 64); SB();
      const u32x4 R12_r = lds_ld<u32x4>(ra1 + 128); SB();
      const u32x4 R20_r = lds_ld<u32x4>(ra2 + 0); SB();
      auto cl00 = cl00_r; auto cl01 = cl01_r; auto cl02 = cl02_r; auto ch00 = ch00_r; auto ch01 = ch01_r; auto ch02 = ch02_r; asm volatile("" : "+v"(cl00), "+v"(cl01), "+v"(cl02), "+v"(ch00), "+v"(ch01), "+v"(ch02)); SB();
      const f32x4 cs0_k0 = mfma(ONES, u32x4{cl00[0], cl00[1], ch00[0], ch00[1]}, Z4); SB();
      const u32x4 R21_r = lds_ld<u32x4>(ra2 + 64); SB();
      const u32x4 R22_r = lds_ld<u32x4>(ra2 + 128); SB();
      const f32x4 cs0_k1 = mfma(ONES, u32x4{cl01[0], cl01[1], ch01[0], ch01[1]}, cs0_k0); SB();
      auto cl10 = cl10_r; auto cl11 = cl11_r; auto cl12 = cl12_r; auto ch10 = ch10_r; auto ch11 = ch11_r; auto ch12 = ch12_r; asm volatile("" : "+v"(cl10), "+v"(cl11), "+v"(cl12), "+v"(ch10), "+v"(ch11), "+v"(ch12)); SB();
      const f32x4 cs0 = mfma(ONES, u32x4{cl02[0], cl02[1], ch02[0], ch02[1]}, cs0_k1); SB();
      const f32x4 cs1_k0 = mfma(ONES, u32x4{cl10[0], cl10[1], ch10[0], ch10[1]}, Z4); SB();
      auto cl20 = cl20_r; auto cl21 = cl21_r; auto cl22 = cl22_r; auto ch20 = ch20_r; auto ch21 = ch21_r; auto ch22 = ch22_r; asm volatile("" : "+v"(cl20), "+v"(cl21), "+v"(cl22), "+v"(ch20), "+v"(ch21), "+v"(ch22)); SB();
      const f32x4 cs1_k1 = mfma(ONES, u32x4{cl11[0], cl11[1], ch11[0], ch11[1]}, cs1_k0); SB();
      const f32x4 cs1 = mfma(ONES, u32x4{cl12[0], cl12[1], ch12[0], ch12[1]}, cs1_k1); SB();
      auto cl30 = cl30_r; auto cl31 = cl31_r; auto cl32 = cl32_r; auto ch30 = ch30_r; auto ch31 = ch31_r; auto ch32 = ch32_r; asm volatile("" : "+v"(cl30), "+v"(cl31), "+v"(cl32), "+v"(ch30), "+v"(ch31), "+v"(ch32)); SB();
      const f32x4 cs2_k0 = mfma(ONES, u32x4{cl20[0], cl20[1], ch20[0], ch20[1]}, Z4); SB();
      const f32x4 cs2_k1 = mfma(ONES, u32x4{cl21[0], cl21[1], ch21[0], ch21[1]}, cs2_k0); SB();
      const f32x4 cs2 = mfma(ONES, u32x4{cl22[0], cl22[1], ch22[0], ch22[1]}, cs2_k1); SB();
      auto cl40 = cl40_r; auto cl41 = cl41_r; auto cl42 = cl42_r; auto ch40 = ch40_r; auto ch41 = ch41_r; auto ch42 = ch42_r; asm volatile("" : "+v"(cl40), "+v"(cl41), "+v"(cl42), "+v"(ch40), "+v"(ch41), "+v"(ch42)); SB();
      const f32x4 cs3_k0 = mfma(ONES, u32x4{cl30[0], cl30[1], ch30[0], ch30[1]}, Z4); SB();
      const f32x4 cs3_k1 = mfma(ONES, u32x4{cl31[0], cl31[1], ch31[0], ch31[1]}, cs3_k0); SB();
      const float cm0 = cs0[0] * -INV_C; KEEP(cs0); SB();
      const unsigned cp0 = pk(cm0, 0.f); SB();
      const f32x4 cs3 = mfma(ONES, u32x4{cl32[0], cl32[1], ch32[0], ch32[1]}, cs3_k1); SB();
      lds_st<unsigned short>(ma0, (unsigned short)cp0); SB();
      const float cm1 = cs1[0] * -INV_C; KEEP(cs1); SB();
      const f32x4 cs4_k0 = mfma(ONES, u32x4{cl40[0], cl40[1], ch40[0], ch40[1]}, Z4); SB();
      auto L00 = L00_r; auto L01 = L01_r; auto L02 = L02_r; asm volatile("" : "+v"(L00), "+v"(L01), "+v"(L02)); SB();
      const float cm2 = cs2[0] * -INV_C; KEEP(cs2); SB();
      const f32x4 cs4_k1 = mfma(ONES, u32x4{cl41[0], cl41[1], ch41[0], ch41[1]}, cs4_k0); SB();
      auto R00 = R00_r; auto R01 = R01_r; auto R02 = R02_r; asm volatile("" : "+v"(R00), "+v"(R01), "+v"(R02)); SB();
      const unsigned cp1 = pk(cm1, 0.f); SB();
      const f32x4 cs4 = mfma(ONES, u32x4{cl42[0], cl42[1], ch42[0], ch42[1]}, cs4_k1); SB();
      lds_st<unsigned short>(ma1, (unsigned short)cp1); SB();
      const float cm3 = cs3[0] * -INV_C; KEEP(cs3); SB();
      const f32x4 D00_k0 = mfma(L00, R00, Z4); SB();
      auto L10 = L10_r; auto L11 = L11_r; auto L12 = L12_r; asm volatile("" : "+v"(L10), "+v"(L11), "+v"(L12)); SB();
      const unsigned cp2 = pk(cm2, 0.f); SB();
      const f32x4 D00_k1 = mfma(L01, R01, D00_k0); SB();
      lds_st<unsigned short>(ma2, (unsigned short)cp2); SB();
      const unsigned cp3 = pk(cm3, 0.f); SB();
      const f32x4 D00 = mfma(L02, R02, D00_k1); SB();
      lds_st<unsigned short>(ma3, (unsigned short)cp3); SB();
      const float cm4 = cs4[0] * -INV_C; KEEP(cs4); SB();
      const f32x4 D01_k0 = mfma(L10, R00, Z4); SB();
      auto L20 = L20_r; auto L21 = L21_r; auto L22 = L22_r; asm volatile("" : "+v"(L20), "+v"(L21), "+v"(L22)); SB();
      const unsigned cp4 = pk(cm4, 0.f); SB();
      const f32x4 D01_k1 = mfma(L11, R01, D01_k0); SB();
      lds_st<unsigned short>(ma4, (unsigned short)cp4); SB();
      const f32x4 D01 = mfma(L12, R02, D01_k1); SB();
      auto L30 = L30_r; auto L31 = L31_r; auto L32 = L32_r; asm volatile("" : "+v"(L30), "+v"(L31), "+v"(L32)); SB();
      const f32x4 D02_k0 = mfma(L20, R00, Z4); SB();
      const f32x4 D02_k1 = mfma(L21, R01, D02_k0); SB();
      const f32x4 D02 = mfma(L22, R02, D02_k1); SB();
      auto L40 = L40_r; auto L41 = L41_r; auto L42 = L42_r; asm volatile("" : "+v"(L40), "+v"(L41), "+v"(L42)); SB();
      const f32x4 D03_k0 = mfma(L30, R00, Z4); SB();
      const f32x4 D03_k1 = mfma(L31, R01, D03_k0); SB();
      const f32x4 D03 = mfma(L32, R02, D03_k1); SB();
      auto L50 = L50_r; auto L51 = L51_r; auto L52 = L52_r; asm volatile("" : "+v"(L50), "+v"(L51), "+v"(L52)); SB();
      const f32x4 D04_k0 = mfma(L40, R00, Z4); SB();
      const f32x4 D04_k1 = mfma(L41, R01, D04_k0); SB();
      flag_arrive(fP2); SB();
      const f32x4 D04 = mfma(L42, R02, D04_k1); SB();
      auto R10 = R10_r; auto R11 = R11_r; auto R12 = R12_r; asm volatile("" : "+v"(R10), "+v"(R11), "+v"(R12)); SB();
      const f32x4 D05_k0 = mfma(L50, R00, Z4); SB();
      const f32x4 D05_k1 = mfma(L51, R01, D05_k0); SB();
      const f32x4 D05 = mfma(L52, R02, D05_k1); SB();
      const f32x4 D10_k0 = mfma(L00, R10, Z4); SB();
      const f32x4 D10_k1 = mfma(L01, R11, D10_k0); SB();
      const f32x4 D10 = mfma(L02, R12, D10_k1); SB();
      const f32x4 D11_k0 = mfma(L10, R10, Z4); SB();
      const f32x4 D11_k1 = mfma(L11, R11, D11_k0); SB();
      const f32x4 D11 = mfma(L12, R12, D11_k1); SB();
      const f32x4 D12_k0 = mfma(L20, R10, Z4); SB();
      const f32x4 D12_k1 = mfma(L21, R11, D12_k0); SB();
      const f32x4 D12 = mfma(L22, R12, D12_k1); SB();
      const f32x4 D13_k0 = mfma(L30, R10, Z4); SB();
      const f32x4 D13_k1 = mfma(L31, R11, D13_k0); SB();
      const f32x4 D13 = mfma(L32, R12, D13_k1); SB();
      const f32x4 D14_k0 = mfma(L40, R10, Z4); SB();
      const f32x4 D14_k1 = mfma(L41, R11, D14_k0); SB();
      const f32x4 D14 = mfma(L42, R12, D14_k1); SB();
      auto R20 = R20_r; auto R21 = R21_r; auto R22 = R22_r; asm volatile("" : "+v"(R20), "+v"(R21), "+v"(R22)); SB();
      const f32x4 D15_k0 = mfma(L50, R10, Z4); SB();
      const f32x4 D15_k1 = mfma(L51, R11, D15_k0); SB();
      const f32x4 D15 = mfma(L52, R12, D15_k1); SB();
      flag_wait(fP2, 4); XDMA(0); SB();
      const f32x4 D20_k0 = mfma(L00, R20, Z4); SB();
      const u32x4 MQ0_r = lds_ld<u32x4>(mub + 0); SB();
      const u32x4 MQ1_r = lds_ld<u32x4>(mub + 64); SB();
      const f32x4 D20_k1 = mfma(L01, R21, D20_k0); SB();
      const u32x4 MQ2_r = lds_ld<u32x4>(mub + 128); SB();
      const u32x4 MK0_r = lds_ld<u32x4>(mub + 192); SB();
      const f32x4 D20 = mfma(L02, R22, D20_k1); SB();
      const u32x4 MK1_r = lds_ld<u32x4>(mub + 256); SB();
      const u32x4 MK2_r = lds_ld<u32x4>(mub + 320); SB();
      const f32x4 D21_k0 = mfma(L10, R20, Z4); SB();
      const f32x4 D21_k1 = mfma(L11, R21, D21_k0); SB();
      const f32x4 D21 = mfma(L12, R22, D21_k1); SB();
      XDMA(1); SB();
      const f32x4 D22_k0 = mfma(L20, R20, Z4); SB();
      const f32x4 D22_k1 = mfma(L21, R21, D22_k0); SB();
      const f32x4 D22 = mfma(L22, R22, D22_k1); SB();
      const f32x4 D23_k0 = mfma(L30, R20, Z4); SB();
      const f32x4 D23_k1 = mfma(L31, R21, D23_k0); SB();
      const f32x4 D23 = mfma(L32, R22, D23_k1); SB();
      XDMA(2); SB();
      const f32x4 D24_k0 = mfma(L40, R20, Z4); SB();
      const f32x4 D24_k1 = mfma(L41, R21, D24_k0); SB();
      const f32x4 D24 = mfma(L42, R22, D24_k1); SB();
      auto MQ0 = MQ0_r; auto MQ1 = MQ1_r; auto MQ2 = MQ2_r; auto MK0 = MK0_r; auto MK1 = MK1_r; auto MK2 = MK2_r; asm volatile("" : "+v"(MQ0), "+v"(MQ1), "+v"(MQ2), "+v"(MK0), "+v"(MK1), "+v"(MK2)); SB();
      const f32x4 D25_k0 = mfma(L50, R20, Z4); SB();
      const f32x4 D25_k1 = mfma(L51, R21, D25_k0); SB();
      const f32x4 D25 = mfma(L52, R22, D25_k1); SB();
      const f32x4 Tg_k0 = mfma(MQ0, MK0, Z4); SB();
      const f32x4 Tg_k1 = mfma(MQ1, MK1, Tg_k0); SB();
      const f32x4 Tg = mfma(MQ2, MK2, Tg_k1); SB();
      const f32x4 Db0_k0 = mfma(L00, MK0, Z4); SB();
      const f32x4 Db0_k1 = mfma(L01, MK1, Db0_k0); SB();
      const f32x4 Db0 = mfma(L02, MK2, Db0_k1); SB();
      const f32x4 Db1_k0 = mfma(L10, MK0, Z4); SB();
      const f32x4 Db1_k1 = mfma(L11, MK1, Db1_k0); SB();
      const f32x4 Db1 = mfma(L12, MK2, Db1_k1); SB();
      const f32x4 Ta0_k0 = mfma(MQ0, R00, Tg); SB();
      const f32x4 Ta0_k1 = mfma(MQ1, R01, Ta0_k0); SB();
      const f32x4 Ta0 = mfma(MQ2, R02, Ta0_k1); SB();
      const f32x4 Db2_k0 = mfma(L20, MK0, Z4); SB();
      const f32x4 Db2_k1 = mfma(L21, MK1, Db2_k0); SB();
      const float bs00 = Db0[0] * scale; SB();
      const float bs01 = Db0[1] * scale; SB();
      const f32x4 Db2 = mfma(L22, MK2, Db2_k1); SB();
      const float bs02 = Db0[2] * scale; SB();
      const float bs03 = Db0[3] * scale; SB();
      const f32x4 Db3_k0 = mfma(L30, MK0, Z4); SB();
      const float f000 = fmaf(D00[0], scale, bs00); SB();
      const float f001 = fmaf(D00[1], scale, bs01); SB();
      const f32x4 Db3_k1 = mfma(L31, MK1, Db3_k0); SB();
      const float f002 = fmaf(D00[2], scale, bs02); SB();
      const float f003 = fmaf(D00[3], scale, bs03); SB();
      const f32x4 Db3 = mfma(L32, MK2, Db3_k1); SB();
      const float f100 = fmaf(D10[0], scale, bs00); SB();
      const float f101 = fmaf(D10[1], scale, bs01); SB();
      const f32x4 Ta1_k0 = mfma(MQ0, R10, Tg); SB();
      const float f102 = fmaf(D10[2], scale, bs02); SB();
      const float f103 = fmaf(D10[3], scale, bs03); SB();
      const f32x4 Ta1_k1 = mfma(MQ1, R11, Ta1_k0); SB();
      const float f200 = fmaf(D20[0], scale, bs00); SB();
      const float f201 = fmaf(D20[1], scale, bs01); SB();
      const f32x4 Ta1 = mfma(MQ2, R12, Ta1_k1); SB();
      const float f202 = fmaf(D20[2], scale, bs02); SB();
      const float f203 = fmaf(D20[3], scale, bs03); SB();
      const f32x4 Db4_k0 = mfma(L40, MK0, Z4); SB();
      const float bs10 = Db1[0] * scale; SB();
      const float bs11 = Db1[1] * scale; SB();
      const f32x4 Db4_k1 = mfma(L41, MK1, Db4_k0); SB();
      const float bs12 = Db1[2] * scale; SB();
      const float bs13 = Db1[3] * scale; SB();
      const f32x4 Db4 = mfma(L42, MK2, Db4_k1); SB();
      const float f010 = fmaf(D01[0], scale, bs10); SB();
      const float f011 = fmaf(D01[1], scale, bs11); SB();
      const f32x4 Db5_k0 = mfma(L50, MK0, Z4); SB();
      const float f012 = fmaf(D01[2], scale, bs12); SB();
      const float f013 = fmaf(D01[3], scale, bs13); SB();
      const f32x4 Db5_k1 = mfma(L51, MK1, Db5_k0); SB();
      const float f110 = fmaf(D11[0], scale, bs10); SB();
      const float f111 = fmaf(D11[1], scale, bs11); SB();
      const f32x4 Db5 = mfma(L52, MK2, Db5_k1); SB();
      const float f112 = fmaf(D11[2], scale, bs12); SB();
      const float f113 = fmaf(D11[3], scale, bs13); SB();
      const f32x4 Ta2_k0 = mfma(MQ0, R20, Tg); SB();
      const float f210 = fmaf(D21[0], scale, bs10); SB();
      const float f211 = fmaf(D21[1], scale, bs11); SB();
      const f32x4 Ta2_k1 = mfma(MQ1, R21, Ta2_k0); SB();
      const float f212 = fmaf(D21[2], scale, bs12); SB();
      const float f213 = fmaf(D21[3], scale, bs13); SB();
      const f32x4 Ta2 = mfma(MQ2, R22, Ta2_k1); SB();
      const float ts0 = Ta0[0] * scale; KEEP(Ta0); SB();
      const float h000 = f000 + ts0; SB();
      const float h001 = f001 + ts0; SB();
      const float h002 = f002 + ts0; SB();
      const float h003 = f003 + ts0; SB();
      const float h010 = f010 + ts0; SB();
      const float h011 = f011 + ts0; SB();
      const float h012 = f012 + ts0; SB();
      const float h013 = f013 + ts0; SB();
      const unsigned k000 = pk(h000, h001); SB();
      const unsigned k002 = pk(h002, h003); SB();
      const unsigned k010 = pk(h010, h011); SB();
      const unsigned k012 = pk(h012, h013); SB();
      const float bs20 = Db2[0] * scale; SB();
      const float bs21 = Db2[1] * scale; SB();
      const float bs22 = Db2[2] * scale; SB();
      const float bs23 = Db2[3] * scale; SB();
      const float f020 = fmaf(D02[0], scale, bs20); SB();
      const float f021 = fmaf(D02[1], scale, bs21); SB();
      const float f022 = fmaf(D02[2], scale, bs22); SB();
      const float f023 = fmaf(D02[3], scale, bs23); SB();
      const float h020 = f020 + ts0; SB();
      const float h021 = f021 + ts0; SB();
      const float h022 = f022 + ts0; SB();
      const float h023 = f023 + ts0; SB();
      const float f120 = fmaf(D12[0], scale, bs20); SB();
      const float f121 = fmaf(D12[1], scale, bs21); SB();
      const float f122 = fmaf(D12[2], scale, bs22); SB();
      const float f123 = fmaf(D12[3], scale, bs23); SB();
      const float f220 = fmaf(D22[0], scale, bs20); SB();
      const float f221 = fmaf(D22[1], scale, bs21); SB();
      const float f222 = fmaf(D22[2], scale, bs22); SB();
      const float f223 = fmaf(D22[3], scale, bs23); SB();
      const unsigned k020 = pk(h020, h021); SB();
      const unsigned k022 = pk(h022, h023); SB();
      const float bs30 = Db3[0] * scale; SB();
      const float bs31 = Db3[1] * scale; SB();
      const float bs32 = Db3[2] * scale; SB();
      const float bs33 = Db3[3] * scale; SB();
      const float f030 = fmaf(D03[0], scale, bs30); SB();
      const float f031 = fmaf(D03[1], scale, bs31); SB();
      const float f032 = fmaf(D03[2], scale, bs32); SB();
      const float f033 = fmaf(D03[3], scale, bs33); SB();
      const float h030 = f030 + ts0; SB();
      const float h031 = f031 + ts0; SB();
      const float h032 = f032 + ts0; SB();
      const float h033 = f033 + ts0; SB();
      const unsigned k030 = pk(h030, h031); SB();
      const unsigned k032 = pk(h032, h033); SB();
      const float f130 = fmaf(D13[0], scale, bs30); SB();
      const float f131 = fmaf(D13[1], scale, bs31); SB();
      const float f132 = fmaf(D13[2], scale, bs32); SB();
      const float f133 = fmaf(D13[3], scale, bs33); SB();
      const float f230 = fmaf(D23[0], scale, bs30); SB();
      const float f231 = fmaf(D23[1], scale, bs31); SB();
      const float f232 = fmaf(D23[2], scale, bs32); SB();
      const float f233 = fmaf(D23[3], scale, bs33); SB();
      const float ts1 = Ta1[0] * scale; KEEP(Ta1); SB();
      const float h100 = f100 + ts1; SB();
      const float h101 = f101 + ts1; SB();
      const float h102 = f102 + ts1; SB();
      const float h103 = f103 + ts1; SB();
      const float h110 = f110 + ts1; SB();
      const float h111 = f111 + ts1; SB();
      const float h112 = f112 + ts1; SB();
      const float h113 = f113 + ts1; SB();
      const float h120 = f120 + ts1; SB();
      const float h121 = f121 + ts1; SB();
      const float h122 = f122 + ts1; SB();
      const float h123 = f123 + ts1; SB();
      const float h130 = f130 + ts1; SB();
      const float h131 = f131 + ts1; SB();
      const float h132 = f132 + ts1; SB();
      const float h133 = f133 + ts1; SB();
      const unsigned k100 = pk(h100, h101); SB();
      const unsigned k102 = pk(h102, h103); SB();
      const unsigned k110 = pk(h110, h111); SB();
      const unsigned k112 = pk(h112, h113); SB();
      const unsigned k120 = pk(h120, h121); SB();
      const unsigned k122 = pk(h122, h123); SB();
      const unsigned k130 = pk(h130, h131); SB();
      const unsigned k132 = pk(h132, h133); SB();
      const float bs40 = Db4[0] * scale; SB();
      const float bs41 = Db4[1] * scale; SB();
      const float bs42 = Db4[2] * scale; SB();
      const float bs43 = Db4[3] * scale; SB();
      const float f040 = fmaf(D04[0], scale, bs40); SB();
      const float f041 = fmaf(D04[1], scale, bs41); SB();
      const float f042 = fmaf(D04[2], scale, bs42); SB();
      const float f043 = fmaf(D04[3], scale, bs43); SB();
      const float f140 = fmaf(D14[0], scale, bs40); SB();
      const float f141 = fmaf(D14[1], scale, bs41); SB();
      const float f142 = fmaf(D14[2], scale, bs42); SB();
      const float f143 = fmaf(D14[3], scale, bs43); SB();
      const float h040 = f040 + ts0; SB();
      const float h041 = f041 + ts0; SB();
      const float h042 = f042 + ts0; SB();
      const float h043 = f043 + ts0; SB();
      const float h140 = f140 + ts1; SB();
      const float h141 = f141 + ts1; SB();
      const float h142 = f142 + ts1; SB();
      const float h143 = f143 + ts1; SB();
      const float f240 = fmaf(D24[0], scale, bs40); SB();
      const float f241 = fmaf(D24[1], scale, bs41); SB();
      const float f242 = fmaf(D24[2], scale, bs42); SB();
      const float f243 = fmaf(D24[3], scale, bs43); SB();
      const unsigned k040 = pk(h040, h041); SB();
      const unsigned k042 = pk(h042, h043); SB();
      const unsigned k140 = pk(h140, h141); SB();
      const unsigned k142 = pk(h142, h143); SB();
      const float bs50 = Db5[0] * scale; SB();
      const float bs51 = Db5[1] * scale; SB();
      const float bs52 = Db5[2] * scale; SB();
      const float bs53 = Db5[3] * scale; SB();
      const float f050 = fmaf(D05[0], scale, bs50); SB();
      const float f051 = fmaf(D05[1], scale, bs51); SB();
      const float f052 = fmaf(D05[2], scale, bs52); SB();
      const float f053 = fmaf(D05[3], scale, bs53); SB();
      const float f150 = fmaf(D15[0], scale, bs50); SB();
      const float f151 = fmaf(D15[1], scale, bs51); SB();
      const float f152 = fmaf(D15[2], scale, bs52); SB();
      const float f153 = fmaf(D15[3], scale, bs53); SB();
      const float h050 = f050 + ts0; SB();
      const float h051 = f051 + ts0; SB();
      const float h052 = f052 + ts0; SB();
      const float h053 = f053 + ts0; SB();
      const float h150 = f150 + ts1; SB();
      const float h151 = f151 + ts1; SB();
      const float h152 = f152 + ts1; SB();
      const float h153 = f153 + ts1; SB();
      const unsigned k050 = pk(h050, h051); SB();
      const unsigned k052 = pk(h052, h053); SB();
      const unsigned k150 = pk(h150, h151); SB();
      const unsigned k152 = pk(h152, h153); SB();
      const float f250 = fmaf(D25[0], scale, bs50); SB();
      const float f251 = fmaf(D25[1], scale, bs51); SB();
      const float f252 = fmaf(D25[2], scale, bs52); SB();
      const float f253 = fmaf(D25[3], scale, bs53); SB();
      const float ts2 = Ta2[0] * scale; KEEP(Ta2); SB();
      const float h200 = f200 + ts2; SB();
      const float h201 = f201 + ts2; SB();
      const float h202 = f202 + ts2; SB();
      const float h203 = f203 + ts2; SB();
      const float h210 = f210 + ts2; SB();
      const float h211 = f211 + ts2; SB();
      const float h212 = f212 + ts2; SB();
      const float h213 = f213 + ts2; SB();
      const float h220 = f220 + ts2; SB();
      const float h221 = f221 + ts2; SB();
      const float h222 = f222 + ts2; SB();
      const float h223 = f223 + ts2; SB();
      const float h230 = f230 + ts2; SB();
      const float h231 = f231 + ts2; SB();
      const float h232 = f232 + ts2; SB();
      const float h233 = f233 + ts2; SB();
      const float h240 = f240 + ts2; SB();
      const float h241 = f241 + ts2; SB();
      const float h242 = f242 + ts2; SB();
      const float h243 = f243 + ts2; SB();
      const float h250 = f250 + ts2; SB();
      const float h251 = f251 + ts2; SB();
      const float h252 = f252 + ts2; SB();
      const float h253 = f253 + ts2; SB();
      const unsigned k200 = pk(h200, h201); SB();
      const unsigned k202 = pk(h202, h203); SB();
      const unsigned k210 = pk(h210, h211); SB();
      const unsigned k212 = pk(h212, h213); SB();
      const unsigned k220 = pk(h220, h221); SB();
      const unsigned k222 = pk(h222, h223); SB();
      const unsigned k230 = pk(h230, h231); SB();
      const unsigned k232 = pk(h232, h233); SB();
      const unsigned k240 = pk(h240, h241); SB();
      const unsigned k242 = pk(h242, h243); SB();
      const unsigned k250 = pk(h250, h251); SB();
      const unsigned k252 = pk(h252, h253); SB();
      lds_st<u32x4>(wdst + 0, u32x4{k000, k002, k010, k012}); SB();
      lds_st<u32x4>(wdst + 1024, u32x4{k020, k022, k030, k032}); SB();
      lds_st<u32x4>(wdst + 3072, u32x4{k100, k102, k110, k112}); SB();
      lds_st<u32x4>(wdst + 4096, u32x4{k120, k122, k130, k132}); SB();
      lds_st<u32x4>(wdst + 2048, u32x4{k040, k042, k050, k052}); SB();
      lds_st<u32x4>(wdst + 5120, u32x4{k140, k142, k150, k152}); SB();
      lds_st<u32x4>(wdst + 6144, u32x4{k200, k202, k210, k212}); SB();
      lds_st<u32x4>(wdst + 7168, u32x4{k220, k222, k230, k232}); SB();
      lds_st<u32x4>(wdst + 8192, u32x4{k240, k242, k250, k252}); SB();
    } else {
      SB();
      const u32x2 cl00_r = lds_tr(ca0 + 0); SB();
      const u32x2 ch00_r = lds_tr(ca0 + 768); SB();
      const u32x2 cl01_r = lds_tr(ca0 + 6144); SB();
      const u32x2 ch01_r = lds_tr(ca0 + 6912); SB();
      const u32x2 cl02_r = lds_tr(ca0 + 12288); SB();
      const u32x2 ch02_r = lds_tr(ca0 + 13056); SB();
      const u32x2 cl10_r = lds_tr(ca1 + 0); SB();
      const u32x2 ch10_r = lds_tr(ca1 + 768); SB();
      const u32x2 cl11_r = lds_tr(ca1 + 6144); SB();
      const u32x2 ch11_r = lds_tr(ca1 + 6912); SB();
      const u32x2 cl12_r = lds_tr(ca1 + 12288); SB();
      const u32x2 ch12_r = lds_tr(ca1 + 13056); SB();
      const u32x2 cl20_r = lds_tr(ca2 + 0); SB();
      const u32x2 ch20_r = lds_tr(ca2 + 768); SB();
      const u32x2 cl21_r = lds_tr(ca2 + 6144); SB();
      const u32x2 ch21_r = lds_tr(ca2 + 6912); SB();
      const u32x2 cl22_r = lds_tr(ca2 + 12288); SB();
      const u32x2 ch22_r = lds_tr(ca2 + 13056); SB();
      const u32x2 cl30_r = lds_tr(ca3 + 0); SB();
      const u32x2 ch30_r = lds_tr(ca3 + 768); SB();
      const u32x2 cl31_r = lds_tr(ca3 + 6144); SB();
      const u32x2 ch31_r = lds_tr(ca3 + 6912); SB();
      const u32x2 cl32_r = lds_tr(ca3 + 12288); SB();
      const u32x2 ch32_r = lds_tr(ca3 + 13056); SB();
      const u32x2 cl40_r = lds_tr(ca4 + 0); SB();
      const u32x2 ch40_r = lds_tr(ca4 + 768); SB();
      const u32x2 cl41_r = lds_tr(ca4 + 6144); SB();
      const u32x2 ch41_r = lds_tr(ca4 + 6912); SB();
      const u32x2 cl42_r = lds_tr(ca4 + 12288); SB();
      const u32x2 ch42_r = lds_tr(ca4 + 13056); SB();
      const u32x2 Rl00 = lds_tr(ra0 + 0); SB();
      const u32x2 Rh00 = lds_tr(ra0 + 768); SB();
      const u32x2 Rl01 = lds_tr(ra0 + 6144); SB();
      const u32x2 Rh01 = lds_tr(ra0 + 6912); SB();
      const u32x2 Rl02 = lds_tr(ra0 + 12288); SB();
      const u32x2 Rh02 = lds_tr(ra0 + 13056); SB();
      const u32x4 L00_r = lds_ld<u32x4>(la + 0); SB();
      const u32x4 L01_r = lds_ld<u32x4>(la + 64); SB();
      const u32x4 L02_r = lds_ld<u32x4>(la + 128); SB();
      const u32x4 L10_r = lds_ld<u32x4>(la + 768); SB();
      const u32x4 L11_r = lds_ld<u32x4>(la + 832); SB();
      const u32x4 L12_r = lds_ld<u32x4>(la + 896); SB();
      const u32x4 L20_r = lds_ld<u32x4>(la + 6144); SB();
      const u32x4 L21_r = lds_ld<u32x4>(la + 6208); SB();
      const u32x4 L22_r = lds_ld<u32x4>(la + 6272); SB();
      const u32x4 L30_r = lds_ld<u32x4>(la + 6912); SB();
      const u32x4 L31_r = lds_ld<u32x4>(la + 6976); SB();
      const u32x4 L32_r = lds_ld<u32x4>(la + 7040); SB();
      const u32x4 L40_r = lds_ld<u32x4>(la + 12288); SB();
      const u32x4 L41_r = lds_ld<u32x4>(la + 12352); SB();
      const u32x4 L42_r = lds_ld<u32x4>(la + 12416); SB();
      const u32x4 L50_r = lds_ld<u32x4>(la + 13056); SB();
      const u32x4 L51_r = lds_ld<u32x4>(la + 13120); SB();
      const u32x4 L52_r = lds_ld<u32x4>(la + 13184); SB();
      const u32x2 Rl10 = lds_tr(ra1 + 0); SB();
      auto cl00 = cl00_r; auto cl01 = cl01_r; auto cl02 = cl02_r; auto ch00 = ch00_r; auto ch01 = ch01_r; auto ch02 = ch02_r; asm volatile("" : "+v"(cl00), "+v"(cl01), "+v"(cl02), "+v"(ch00), "+v"(ch01), "+v"(ch02)); SB();
      const f32x4 cs0_k0 = mfma(ONES, u32x4{cl00[0], cl00[1], ch00[0], ch00[1]}, Z4); SB();
      const u32x2 Rh10 = lds_tr(ra1 + 768); SB();
      const u32x2 Rl11 = lds_tr(ra1 + 6144); SB();
      const f32x4 cs0_k1 = mfma(ONES, u32x4{cl01[0], cl01[1], ch01[0], ch01[1]}, cs0_k0); SB();
      auto cl10 = cl10_r; auto cl11 = cl11_r; auto cl12 = cl12_r; auto ch10 = ch10_r; auto ch11 = ch11_r; auto ch12 = ch12_r; asm volatile("" : "+v"(cl10), "+v"(cl11), "+v"(cl12), "+v"(ch10), "+v"(ch11), "+v"(ch12)); SB();
      const u32x2 Rh11 = lds_tr(ra1 + 6912); SB();
      const f32x4 cs0 = mfma(ONES, u32x4{cl02[0], cl02[1], ch02[0], ch02[1]}, cs0_k1); SB();
      const u32x2 Rl12 = lds_tr(ra1 + 12288); SB();
      auto cl20 = cl20_r; auto cl21 = cl21_r; auto cl22 = cl22_r; auto ch20 = ch20_r; auto ch21 = ch21_r; auto ch22 = ch22_r; asm volatile("" : "+v"(cl20), "+v"(cl21), "+v"(cl22), "+v"(ch20), "+v"(ch21), "+v"(ch22)); SB();
      const f32x4 cs1_k0 = mfma(ONES, u32x4{cl10[0], cl10[1], ch10[0], ch10[1]}, Z4); SB();
      const u32x2 Rh12 = lds_tr(ra1 + 13056); SB();
      const u32x2 Rl20 = lds_tr(ra2 + 0); SB();
      const f32x4 cs1_k1 = mfma(ONES, u32x4{cl11[0], cl11[1], ch11[0], ch11[1]}, cs1_k0); SB();
      const u32x2 Rh20 = lds_tr(ra2 + 768); SB();
      const u32x2 Rl21 = lds_tr(ra2 + 6144); SB();
      const f32x4 cs1 = mfma(ONES, u32x4{cl12[0], cl12[1], ch12[0], ch12[1]}, cs1_k1); SB();
      auto cl30 = cl30_r; auto cl31 = cl31_r; auto cl32 = cl32_r; auto ch30 = ch30_r; auto ch31 = ch31_r; auto ch32 = ch32_r; asm volatile("" : "+v"(cl30), "+v"(cl31), "+v"(cl32), "+v"(ch30), "+v"(ch31), "+v"(ch32)); SB();
      const float cm0 = cs0[0] * -INV_C; KEEP(cs0); SB();
      const f32x4 cs2_k0 = mfma(ONES, u32x4{cl20[0], cl20[1], ch20[0], ch20[1]}, Z4); SB();
      const u32x4 R00_r = u32x4{Rl00[0], Rl00[1], Rh00[0], Rh00[1]};
      const u32x2 Rh21 = lds_tr(ra2 + 6912); SB();
      const u32x4 R01_r = u32x4{Rl01[0], Rl01[1], Rh01[0], Rh01[1]};
      const unsigned cp0 = pk(cm0, 0.f); SB();
      const f32x4 cs2_k1 = mfma(ONES, u32x4{cl21[0], cl21[1], ch21[0], ch21[1]}, cs2_k0); SB();
      const u32x4 R02_r = u32x4{Rl02[0], Rl02[1], Rh02[0], Rh02[1]};
      lds_st<unsigned short>(ma0, (unsigned short)cp0); SB();
      const u32x2 Rl22 = lds_tr(ra2 + 12288); SB();
      const f32x4 cs2 = mfma(ONES, u32x4{cl22[0], cl22[1], ch22[0], ch22[1]}, cs2_k1); SB();
      auto cl40 = cl40_r; auto cl41 = cl41_r; auto cl42 = cl42_r; auto ch40 = ch40_r; auto ch41 = ch41_r; auto ch42 = ch42_r; asm volatile("" : "+v"(cl40), "+v"(cl41), "+v"(cl42), "+v"(ch40), "+v"(ch41), "+v"(ch42)); SB();
      const float cm1 = cs1[0] * -INV_C; KEEP(cs1); SB();
      const f32x4 cs3_k0 = mfma(ONES, u32x4{cl30[0], cl30[1], ch30[0], ch30[1]}, Z4); SB();
      const u32x2 Rh22 = lds_tr(ra2 + 13056); SB();
      const unsigned cp1 = pk(cm1, 0.f); SB();
      const f32x4 cs3_k1 = mfma(ONES, u32x4{cl31[0], cl31[1], ch31[0], ch31[1]}, cs3_k0); SB();
      lds_st<unsigned short>(ma1, (unsigned short)cp1); SB();
      const f32x4 cs3 = mfma(ONES, u32x4{cl32[0], cl32[1], ch32[0], ch32[1]}, cs3_k1); SB();
      auto L00 = L00_r; auto L01 = L01_r; auto L02 = L02_r; asm volatile("" : "+v"(L00), "+v"(L01), "+v"(L02)); SB();
      auto R00 = R00_r; auto R01 = R01_r; auto R02 = R02_r; asm volatile("" : "+v"(R00), "+v"(R01), "+v"(R02)); SB();
      const f32x4 cs4_k0 = mfma(ONES, u32x4{cl40[0], cl40[1], ch40[0], ch40[1]}, Z4); SB();
      const u32x4 R10_r = u32x4{Rl10[0], Rl10[1], Rh10[0], Rh10[1]};
      const float cm2 = cs2[0] * -INV_C; KEEP(cs2); SB();
      const unsigned cp2 = pk(cm2, 0.f); SB();
      const f32x4 cs4_k1 = mfma(ONES, u32x4{cl41[0], cl41[1], ch41[0], ch41[1]}, cs4_k0); SB();
      lds_st<unsigned short>(ma2, (unsigned short)cp2); SB();
      const u32x4 R11_r = u32x4{Rl11[0], Rl11[1], Rh11[0], Rh11[1]};
      const f32x4 cs4 = mfma(ONES, u32x4{cl42[0], cl42[1], ch42[0], ch42[1]}, cs4_k1); SB();
      auto L10 = L10_r; auto L11 = L11_r; auto L12 = L12_r; asm volatile("" : "+v"(L10), "+v"(L11), "+v"(L12)); SB();
      const f32x4 D00_k0 = mfma(L00, R00, Z4); SB();
      const u32x4 R12_r = u32x4{Rl12[0], Rl12[1], Rh12[0], Rh12[1]};
      const f32x4 D00_k1 = mfma(L01, R01, D00_k0); SB();
      const u32x4 R20_r = u32x4{Rl20[0], Rl20[1], Rh20[0], Rh20[1]};
      const float cm3 = cs3[0] * -INV_C; KEEP(cs3); SB();
      const unsigned cp3 = pk(cm3, 0.f); SB();
      const f32x4 D00 = mfma(L02, R02, D00_k1); SB();
      lds_st<unsigned short>(ma3, (unsigned short)cp3); SB();
      auto L20 = L20_r; auto L21 = L21_r; auto L22 = L22_r; asm volatile("" : "+v"(L20), "+v"(L21), "+v"(L22)); SB();
      const f32x4 D01_k0 = mfma(L10, R00, Z4); SB();
      const u32x4 R21_r = u32x4{Rl21[0], Rl21[1], Rh21[0], Rh21[1]};
      const f32x4 D01_k1 = mfma(L11, R01, D01_k0); SB();
      const float cm4 = cs4[0] * -INV_C; KEEP(cs4); SB();
      const unsigned cp4 = pk(cm4, 0.f); SB();
      const f32x4 D01 = mfma(L12, R02, D01_k1); SB();
      lds_st<unsigned short>(ma4, (unsigned short)cp4); SB();
      auto L30 = L30_r; auto L31 = L31_r; auto L32 = L32_r; asm volatile("" : "+v"(L30), "+v"(L31), "+v"(L32)); SB();
      const f32x4 D02_k0 = mfma(L20, R00, Z4); SB();
      const f32x4 D02_k1 = mfma(L21, R01, D02_k0); SB();
      const u32x4 R22_r = u32x4{Rl22[0], Rl22[1], Rh22[0], Rh22[1]};
      const f32x4 D02 = mfma(L22, R02, D02_k1); SB();
      auto L40 = L40_r; auto L41 = L41_r; auto L42 = L42_r; asm volatile("" : "+v"(L40), "+v"(L41), "+v"(L42)); SB();
      const f32x4 D03_k0 = mfma(L30, R00, Z4); SB();
      const f32x4 D03_k1 = mfma(L31, R01, D03_k0); SB();
      const f32x4 D03 = mfma(L32, R02, D03_k1); SB();
      auto L50 = L50_r; auto L51 = L51_r; auto L52 = L52_r; asm volatile("" : "+v"(L50), "+v"(L51), "+v"(L52)); SB();
      const f32x4 D04_k0 = mfma(L40, R00, Z4); SB();
      const f32x4 D04_k1 = mfma(L41, R01, D04_k0); SB();
      flag_arrive(fP2); SB();
      const f32x4 D04 = mfma(L42, R02, D04_k1); SB();
      auto R10 = R10_r; auto R11 = R11_r; auto R12 = R12_r; asm volatile("" : "+v"(R10), "+v"(R11), "+v"(R12)); SB();
      const f32x4 D05_k0 = mfma(L50, R00, Z4); SB();
      const f32x4 D05_k1 = mfma(L51, R01, D05_k0); SB();
      const f32x4 D05 = mfma(L52, R02, D05_k1); SB();
      const f32x4 D10_k0 = mfma(L00, R10, Z4); SB();
      const f32x4 D10_k1 = mfma(L01, R11, D10_k0); SB();
      const f32x4 D10 = mfma(L02, R12, D10_k1); SB();
      const f32x4 D11_k0 = mfma(L10, R10, Z4); SB();
      const f32x4 D11_k1 = mfma(L11, R11, D11_k0); SB();
      const f32x4 D11 = mfma(L12, R12, D11_k1); SB();
      const f32x4 D12_k0 = mfma(L20, R10, Z4); SB();
      const f32x4 D12_k1 = mfma(L21, R11, D12_k0); SB();
      const f32x4 D12 = mfma(L22, R12, D12_k1); SB();
      const f32x4 D13_k0 = mfma(L30, R10, Z4); SB();
      const f32x4 D13_k1 = mfma(L31, R11, D13_k0); SB();
      const f32x4 D13 = mfma(L32, R12, D13_k1); SB();
      const f32x4 D14_k0 = mfma(L40, R10, Z4); SB();
      const f32x4 D14_k1 = mfma(L41, R11, D14_k0); SB();
      const f32x4 D14 = mfma(L42, R12, D14_k1); SB();
      auto R20 = R20_r; auto R21 = R21_r; auto R22 = R22_r; asm volatile("" : "+v"(R20), "+v"(R21), "+v"(R22)); SB();
      const f32x4 D15_k0 = mfma(L50, R10, Z4); SB();
      const f32x4 D15_k1 = mfma(L51, R11, D15_k0); SB();
      const f32x4 D15 = mfma(L52, R12, D15_k1); SB();
      flag_wait(fP2, 4); XDMA(0); SB();
      const f32x4 D20_k0 = mfma(L00, R20, Z4); SB();
      const u32x4 MV0_r = lds_ld<u32x4>(mub + 384); SB();
      const u32x4 MV1_r = lds_ld<u32x4>(mub + 448); SB();
      const f32x4 D20_k1 = mfma(L01, R21, D20_k0); SB();
      const u32x4 MV2_r = lds_ld<u32x4>(mub + 512); SB();
      const f32x4 D20 = mfma(L02, R22, D20_k1); SB();
      const f32x4 D21_k0 = mfma(L10, R20, Z4); SB();
      const f32x4 D21_k1 = mfma(L11, R21, D21_k0); SB();
      const f32x4 D21 = mfma(L12, R22, D21_k1); SB();
      XDMA(1); SB();
      const f32x4 D22_k0 = mfma(L20, R20, Z4); SB();
      const f32x4 D22_k1 = mfma(L21, R21, D22_k0); SB();
      const f32x4 D22 = mfma(L22, R22, D22_k1); SB();
      const f32x4 D23_k0 = mfma(L30, R20, Z4); SB();
      const f32x4 D23_k1 = mfma(L31, R21, D23_k0); SB();
      const f32x4 D23 = mfma(L32, R22, D23_k1); SB();
      XDMA(2); SB();
      const f32x4 D24_k0 = mfma(L40, R20, Z4); SB();
      const f32x4 D24_k1 = mfma(L41, R21, D24_k0); SB();
      const f32x4 D24 = mfma(L42, R22, D24_k1); SB();
      auto MV0 = MV0_r; auto MV1 = MV1_r; auto MV2 = MV2_r; asm volatile("" : "+v"(MV0), "+v"(MV1), "+v"(MV2)); SB();
      const f32x4 D25_k0 = mfma(L50, R20, Z4); SB();
      const f32x4 D25_k1 = mfma(L51, R21, D25_k0); SB();
      const f32x4 D25 = mfma(L52, R22, D25_k1); SB();
      const f32x4 T0_k0 = mfma(MV0, R00, Z4); SB();
      const f32x4 T0_k1 = mfma(MV1, R01, T0_k0); SB();
      const f32x4 T0 = mfma(MV2, R02, T0_k1); SB();
      const f32x4 T1_k0 = mfma(MV0, R10, Z4); SB();
      const f32x4 T1_k1 = mfma(MV1, R11, T1_k0); SB();
      const f32x4 T1 = mfma(MV2, R12, T1_k1); SB();
      const f32x4 T2_k0 = mfma(MV0, R20, Z4); SB();
      const f32x4 T2_k1 = mfma(MV1, R21, T2_k0); SB();
      const f32x4 T2 = mfma(MV2, R22, T2_k1); SB();
      const float h000 = D00[0] + T0[0]; SB();
      const float h001 = D00[1] + T0[0]; SB();
      const float h002 = D00[2] + T0[0]; SB();
      const float h003 = D00[3] + T0[0]; SB();
      const float h010 = D01[0] + T0[0]; SB();
      const float h011 = D01[1] + T0[0]; SB();
      const float h012 = D01[2] + T0[0]; SB();
      const float h013 = D01[3] + T0[0]; SB();
      const float h020 = D02[0] + T0[0]; SB();
      const float h021 = D02[1] + T0[0]; SB();
      const float h022 = D02[2] + T0[0]; SB();
      const float h023 = D02[3] + T0[0]; SB();
      const float h030 = D03[0] + T0[0]; SB();
      const float h031 = D03[1] + T0[0]; SB();
      const float h032 = D03[2] + T0[0]; SB();
      const float h033 = D03[3] + T0[0]; SB();
      const float h040 = D04[0] + T0[0]; SB();
      const float h041 = D04[1] + T0[0]; SB();
      const float h042 = D04[2] + T0[0]; SB();
      const float h043 = D04[3] + T0[0]; SB();
      const float h050 = D05[0] + T0[0]; SB();
      const float h051 = D05[1] + T0[0]; SB();
      const float h052 = D05[2] + T0[0]; SB();
      const float h053 = D05[3] + T0[0]; SB();
      const unsigned k000 = pk(h000, h001); SB();
      const unsigned k002 = pk(h002, h003); SB();
      const unsigned k010 = pk(h010, h011); SB();
      const unsigned k012 = pk(h012, h013); SB();
      const unsigned k020 = pk(h020, h021); SB();
      const unsigned k022 = pk(h022, h023); SB();
      const unsigned k030 = pk(h030, h031); SB();
      const unsigned k032 = pk(h032, h033); SB();
      const unsigned k040 = pk(h040, h041); SB();
      const unsigned k042 = pk(h042, h043); SB();
      const unsigned k050 = pk(h050, h051); SB();
      const unsigned k052 = pk(h052, h053); SB();
      const float h100 = D10[0] + T1[0]; SB();
      const float h101 = D10[1] + T1[0]; SB();
      const float h102 = D10[2] + T1[0]; SB();
      const float h103 = D10[3] + T1[0]; SB();
      const float h110 = D11[0] + T1[0]; SB();
      const float h111 = D11[1] + T1[0]; SB();
      const float h112 = D11[2] + T1[0]; SB();
      const float h113 = D11[3] + T1[0]; SB();
      const float h120 = D12[0] + T1[0]; SB();
      const float h121 = D12[1] + T1[0]; SB();
      const float h122 = D12[2] + T1[0]; SB();
      const float h123 = D12[3] + T1[0]; SB();
      const float h130 = D13[0] + T1[0]; SB();
      const float h131 = D13[1] + T1[0]; SB();
      const float h132 = D13[2] + T1[0]; SB();
      const float h133 = D13[3] + T1[0]; SB();
      const float h140 = D14[0] + T1[0]; SB();
      const float h141 = D14[1] + T1[0]; SB();
      const float h142 = D14[2] + T1[0]; SB();
      const float h143 = D14[3] + T1[0]; SB();
      const float h150 = D15[0] + T1[0]; SB();
      const float h151 = D15[1] + T1[0]; SB();
      const float h152 = D15[2] + T1[0]; SB();
      const float h153 = D15[3] + T1[0]; SB();
      const unsigned k100 = pk(h100, h101); SB();
      const unsigned k102 = pk(h102, h103); SB();
      const unsigned k110 = pk(h110, h111); SB();
      const unsigned k112 = pk(h112, h113); SB();
      const unsigned k120 = pk(h120, h121); SB();
      const unsigned k122 = pk(h122, h123); SB();
      const unsigned k130 = pk(h130, h131); SB();
      const unsigned k132 = pk(h132, h133); SB();
      const unsigned k140 = pk(h140, h141); SB();
      const unsigned k142 = pk(h142, h143); SB();
      const unsigned k150 = pk(h150, h151); SB();
      const unsigned k152 = pk(h152, h153); SB();
      const float h200 = D20[0] + T2[0]; SB();
      const float h201 = D20[1] + T2[0]; SB();
      const float h202 = D20[2] + T2[0]; SB();
      const float h203 = D20[3] + T2[0]; SB();
      const float h210 = D21[0] + T2[0]; SB();
      const float h211 = D21[1] + T2[0]; SB();
      const float h212 = D21[2] + T2[0]; SB();
      const float h213 = D21[3] + T2[0]; SB();
      const float h220 = D22[0] + T2[0]; SB();
      const float h221 = D22[1] + T2[0]; SB();
      const float h222 = D22[2] + T2[0]; SB();
      const float h223 = D22[3] + T2[0]; SB();
      const float h230 = D23[0] + T2[0]; SB();
      const float h231 = D23[1] + T2[0]; SB();
      const float h232 = D23[2] + T2[0]; SB();
      const float h233 = D23[3] + T2[0]; SB();
      const float h240 = D24[0] + T2[0]; SB();
      const float h241 = D24[1] + T2[0]; SB();
      const float h242 = D24[2] + T2[0]; SB();
      const float h243 = D24[3] + T2[0]; SB();
      const float h250 = D25[0] + T2[0]; SB();
      const float h251 = D25[1] + T2[0]; SB();
      const float h252 = D25[2] + T2[0]; SB();
      const float h253 = D25[3] + T2[0]; SB();
      const unsigned k200 = pk(h200, h201); SB();
      const unsigned k202 = pk(h202, h203); SB();
      const unsigned k210 = pk(h210, h211); SB();
      const unsigned k212 = pk(h212, h213); SB();
      const unsigned k220 = pk(h220, h221); SB();
      const unsigned k222 = pk(h222, h223); SB();
      const unsigned k230 = pk(h230, h231); SB();
      const unsigned k232 = pk(h232, h233); SB();
      const unsigned k240 = pk(h240, h241); SB();
      const unsigned k242 = pk(h242, h243); SB();
      const unsigned k250 = pk(h250, h251); SB();
      const unsigned k252 = pk(h252, h253); SB();
      lds_st<u32x4>(wdst + 0, u32x4{k000, k002, k010, k012}); SB();
      lds_st<u32x4>(wdst + 1024, u32x4{k020, k022, k030, k032}); SB();
      lds_st<u32x4>(wdst + 2048, u32x4{k040, k042, k050, k052}); SB();
      lds_st<u32x4>(wdst + 3072, u32x4{k100, k102, k110, k112}); SB();
      lds_st<u32x4>(wdst + 4096, u32x4{k120, k122, k130, k132}); SB();
      lds_st<u32x4>(wdst + 5120, u32x4{k140, k142, k150, k152}); SB();
      lds_st<u32x4>(wdst + 6144, u32x4{k200, k202, k210, k212}); SB();
      lds_st<u32x4>(wdst + 7168, u32x4{k220, k222, k230, k232}); SB();
      lds_st<u32x4>(wdst + 8192, u32x4{k240, k242, k250, k252}); SB();
    }
#undef flag_arrive
    flag_arrive(lb + FLG + 44 + team * 4, lane);
    flag_wait(lb + FLG + 44, 4);
  }

  const int pq = i >> 2, tq = i & 3;
  const bool diag = (g == pq);
  const unsigned idv = diag ? (0x3F80u << (16 * (tq & 1))) : 0u;
  const unsigned ID0 = (tq & 2) ? 0u : idv, ID1 = (tq & 2) ? idv : 0u;
  const unsigned idr = 0x3F80u << (16 * (tq & 1)), IDR0 = (tq & 2) ? 0u : idr, IDR1 = (tq & 2) ? idr : 0u;
  const u32x4 SEL0 = {ID0, ID1, 0u, 0u}, SEL1 = {0u, 0u, ID0, ID1};
  const u32x4 ONES = {0x3F803F80u, 0x3F803F80u, 0x3F803F80u, 0x3F803F80u};
  const f32x4 Z4 = {0.f, 0.f, 0.f, 0.f};
  const unsigned obase = (unsigned)(wy * 7) * ROWB + (unsigned)(wx0 * 7) * TOKB + w * FRAMEB;
  const bool t_odd = tq & 1, t_hi = tq & 2, g_odd = g & 1, g_hi = g & 2, li0 = i == 0;
  const unsigned raE = xt + tq * FS + (12 * w + pq) * TOKB + 16 * (g ^ swz(12 * w + pq)), raO = raE ^ 32;
  const unsigned taE = xt + pq * FS + (12 * w + g) * TOKB + 16 * (tq ^ swz(12 * w + g)), taO = taE ^ 32;
  const int pr1 = (pq + 3) & 3, pr2 = (pq + 2) & 3, pr3 = (pq + 1) & 3;
  const bool diag0 = diag, diag1 = g == pr1, diag2 = g == pr2, diag3 = g == pr3;
  const unsigned rb1 = xt + tq * FS + (12 * w + 4 + pr1) * TOKB + 16 * (g ^ swz(12 * w + 4 + pr1));
  const unsigned rb2t = xt + tq * FS + (12 * w + 8 + pr2) * TOKB + 16 * (g ^ swz(12 * w + 8 + pr2));
  const unsigned r2b = lb + R2_OFF + team * 2048 + tq * 256;
  const unsigned r2a0_0 = r2b + (diag ? 12 * w + pq : 49) * 4, r2a0_1 = r2b + (diag ? 12 * w + 4 + pr1 : 50) * 4, r2a0_2 = r2b + (diag ? 12 * w + 8 + pr2 : 51) * 4;
  const unsigned rb2 = xt + w * FS + i * TOKB + 16 * (g ^ swz(i));
  const unsigned r2f0 = lb + R2_OFF + team * 2048 + w * 256;
  const unsigned pscw = lb + PSC + wave * 128 + lane * 2, pscr = lb + PSC + wave * 128 + g * 8;
  const unsigned fB1 = lb + FLG + team * 8, fB2 = fB1 + 4;
  const float biask = g == 0 ? 0.f : NEG_BIG, biasd = (g == 3 && i != 0) ? NEG_BIG : 0.f;
  auto tok_off = [&](int pos) __attribute__((always_inline)) { return (unsigned)(pos * TOKB + ((pos * 37) >> 8) * (ROWB - 7 * TOKB) + 16 * g); };
  const unsigned so0 = tok_off(i), so1 = tok_off(16 + i), so2 = tok_off(32 + i);
  const unsigned sox = li0 ? 6 * ROWB + 6 * TOKB + 16 * g : 0x7ffffff0u;
#define RA(j) (((j) & 1 ? raO : raE) + 768 * (j))
#define TA(j) (((j) & 1 ? taO : taE) + 768 * (j))
#define flag_arrive(a) flag_arrive(a, lane)
#define DMA_TILE(j) dma_tile<j>(dv[j], nf0, nf0 + FRAMEB, nf0 + 2 * (size_t)FRAMEB, nf0 + 3 * (size_t)FRAMEB, xb, (unsigned)n)
  const char* nf0 = xrow + (size_t)((2 + team) * 7) * TOKB;

#pragma unroll 1
  for (int n = 0; n < 2; ++n) {
    const int wl4 = 2 * n + team;
    if (n == 0) asm volatile("s_waitcnt vmcnt(0)" ::: "memory");
    else        asm volatile("s_waitcnt vmcnt(%0)" :: "i"(STORES_AFTER_DMA) : "memory");
    PRIO_S1();
    {
      constexpr int WMASK = 1;
      const unsigned wl = opaque(lb + W_OFF + lane * 16);
      const unsigned r2a0 = r2a0_0 + n * 1024, r2a1 = r2a0_1 + n * 1024, r2a2 = r2a0_2 + n * 1024;
      const unsigned rb2 = rb2t;
      if (wave == 0 && n == 0) {
        const unsigned raS = lb + SPX + i * TOKB + 16 * g, taS = lb + SPX + (4 * g + pq) * TOKB + 16 * tq;
        const unsigned spgA = lb + SPG + (4 * pr3 + tq) * TOKB + 16 * g, spuA = spgA + (SPU - SPG);
        const unsigned rb3 = lb + SPX + (4 * pr3 + tq) * TOKB + 16 * g;
        const unsigned r2a3 = diag ? lb + SPR + (4 * pr3 + tq) * 4 : lb + PSC;
        const unsigned wl2 = opaque(lb + W_OFF + 36864 + lane * 16);
#define WFRAG2(m, ct, s) lds_ld<u32x4>(wl2 + (((m) * 6 + (ct)) * 3 + (s)) * 1024)
        SB();
      const u32x4 X00_r = lds_ld<u32x4>(RA(0) + 0); SB();
      const u32x4 X01_r = lds_ld<u32x4>(RA(0) + 64); SB();
      const u32x4 X02_r = lds_ld<u32x4>(RA(0) + 128); SB();
      const u32x4 WA00_r = WFRAG(0, 0, 0); SB();
      const u32x4 WA01_r = WFRAG(0, 0, 1); SB();
      const u32x4 WA02_r = WFRAG(0, 0, 2); SB();
      const u32x4 Z10_r = lds_ld<u32x4>(rb1 + 0); SB();
      const u32x4 Z11_r = lds_ld<u32x4>(rb1 + 64); SB();
      const u32x4 Z12_r = lds_ld<u32x4>(rb1 + 128); SB();
      const u32x4 Z20_r = lds_ld<u32x4>(rb2 + 0); SB();
      const u32x4 Z21_r = lds_ld<u32x4>(rb2 + 64); SB();
      const u32x4 Z22_r = lds_ld<u32x4>(rb2 + 128); SB();
      const u32x4 Z30_r = lds_ld<u32x4>(rb3 + 0); SB();
      const u32x4 Z31_r = lds_ld<u32x4>(rb3 + 64); SB();
      const u32x4 Z32_r = lds_ld<u32x4>(rb3 + 128); SB();
      const u32x4 WA10_r = WFRAG(0, 1, 0); SB();
      const u32x4 WA11_r = WFRAG(0, 1, 1); SB();
      const u32x4 WA12_r = WFRAG(0, 1, 2); SB();
      auto X00 = X00_r; auto X01 = X01_r; auto X02 = X02_r; asm volatile("" : "+v"(X00), "+v"(X01), "+v"(X02)); SB();
      const f32x4 G0_k0 = mfma(X00, X00, Z4); SB();
      auto WA00 = WA00_r; auto WA01 = WA01_r; auto WA02 = WA02_r; asm volatile("" : "+v"(WA00), "+v"(WA01), "+v"(WA02)); SB();
      const f32x4 ga00_k0 = mfma(WA00, X00, Z4); SB();
      auto Z10 = Z10_r; auto Z11 = Z11_r; auto Z12 = Z12_r; asm volatile("" : "+v"(Z10), "+v"(Z11), "+v"(Z12)); SB();
      const f32x4 ga01_k0 = mfma(WA00, Z10, Z4); SB();
      auto Z20 = Z20_r; auto Z21 = Z21_r; auto Z22 = Z22_r; asm volatile("" : "+v"(Z20), "+v"(Z21), "+v"(Z22)); SB();
      auto Z30 = Z30_r; auto Z31 = Z31_r; auto Z32 = Z32_r; asm volatile("" : "+v"(Z30), "+v"(Z31), "+v"(Z32)); SB();
      const f32x4 ga02_k0 = mfma(WA00, Z20, Z4); SB();
      const f32x4 ga03_k0 = mfma(WA00, Z30, Z4); SB();
      const f32x4 G0_k1 = mfma(X01, X01, G0_k0); SB();
      const f32x4 ga00_k1 = mfma(WA01, X01, ga00_k0); SB();
      const f32x4 ga01_k1 = mfma(WA01, Z11, ga01_k0); SB();
      const f32x4 ga02_k1 = mfma(WA01, Z21, ga02_k0); SB();
      const f32x4 ga03_k1 = mfma(WA01, Z31, ga03_k0); SB();
      const f32x4 G0 = mfma(X02, X02, G0_k1); SB();
      const f32x4 ga00 = mfma(WA02, X02, ga00_k1); SB();
      const f32x4 ga01 = mfma(WA02, Z12, ga01_k1); SB();
      auto WA10 = WA10_r; auto WA11 = WA11_r; auto WA12 = WA12_r; asm volatile("" : "+v"(WA10), "+v"(WA11), "+v"(WA12)); SB();
      const f32x4 ga02 = mfma(WA02, Z22, ga02_k1); SB();
      const u32x4 X10_r = lds_ld<u32x4>(RA(1) + 0); SB();
      const u32x4 X11_r = lds_ld<u32x4>(RA(1) + 64); SB();
      const f32x4 ga03 = mfma(WA02, Z32, ga03_k1); SB();
      const u32x4 X12_r = lds_ld<u32x4>(RA(1) + 128); SB();
      const u32x4 WA20_r = WFRAG(0, 2, 0); SB();
      const f32x4 Sm0_k0 = mfma(ONES, X00, Z4); SB();
      const u32x4 WA21_r = WFRAG(0, 2, 1); SB();
      const unsigned gp00_0 = pk(ga00[0], ga00[1]); SB();
      const f32x4 ga10_k0 = mfma(WA10, X00, Z4); SB();
      const u32x4 WA22_r = WFRAG(0, 2, 2); SB();
      const unsigned gp00_1 = pk(ga00[2], ga00[3]); SB();
      const f32x4 ga11_k0 = mfma(WA10, Z10, Z4); SB();
      const unsigned gp10_0 = pk(ga01[0], ga01[1]); SB();
      const unsigned gp10_1 = pk(ga01[2], ga01[3]); SB();
      const f32x4 ga12_k0 = mfma(WA10, Z20, Z4); SB();
      const unsigned gp20_0 = pk(ga02[0], ga02[1]); SB();
      const unsigned gp20_1 = pk(ga02[2], ga02[3]); SB();
      const f32x4 ga13_k0 = mfma(WA10, Z30, Z4); SB();
      const unsigned gp30_0 = pk(ga03[0], ga03[1]); SB();
      const unsigned gp30_1 = pk(ga03[2], ga03[3]); SB();
      const f32x4 Sm0_k1 = mfma(ONES, X01, Sm0_k0); SB();
      const f32x4 ga10_k1 = mfma(WA11, X01, ga10_k0); SB();
      const f32x4 ga11_k1 = mfma(WA11, Z11, ga11_k0); SB();
      const f32x4 ga12_k1 = mfma(WA11, Z21, ga12_k0); SB();
      const f32x4 ga13_k1 = mfma(WA11, Z31, ga13_k0); SB();
      const f32x4 Sm0 = mfma(ONES, X02, Sm0_k1); SB();
      const f32x4 ga10 = mfma(WA12, X02, ga10_k1); SB();
      auto X10 = X10_r; auto X11 = X11_r; auto X12 = X12_r; asm volatile("" : "+v"(X10), "+v"(X11), "+v"(X12)); SB();
      const f32x4 ga11 = mfma(WA12, Z12, ga11_k1); SB();
      auto WA20 = WA20_r; auto WA21 = WA21_r; auto WA22 = WA22_r; asm volatile("" : "+v"(WA20), "+v"(WA21), "+v"(WA22)); SB();
      const f32x4 ga12 = mfma(WA12, Z22, ga12_k1); SB();
      const f32x4 ga13 = mfma(WA12, Z32, ga13_k1); SB();
      const f32x4 G1_k0 = mfma(X10, Z10, Z4); SB();
      const f32x4 ga20_k0 = mfma(WA20, X00, Z4); SB();
      const u32x4 X20_r = lds_ld<u32x4>(RA(2) + 0); SB();
      const u32x4 X21_r = lds_ld<u32x4>(RA(2) + 64); SB();
      const f32x4 ga21_k0 = mfma(WA20, Z10, Z4); SB();
      const u32x4 X22_r = lds_ld<u32x4>(RA(2) + 128); SB();
      const unsigned gp01_0 = pk(ga10[0], ga10[1]); SB();
      const f32x4 G1_k1 = mfma(X11, Z11, G1_k0); SB();
      const u32x4 WA30_r = WFRAG(0, 3, 0); SB();
      const unsigned gp01_1 = pk(ga10[2], ga10[3]); SB();
      const f32x4 ga22_k0 = mfma(WA20, Z20, Z4); SB();
      const u32x4 WA31_r = WFRAG(0, 3, 1); SB();
      const unsigned gp11_0 = pk(ga11[0], ga11[1]); SB();
      const f32x4 ga23_k0 = mfma(WA20, Z30, Z4); SB();
      const u32x4 WA32_r = WFRAG(0, 3, 2); SB();
      const unsigned gp11_1 = pk(ga11[2], ga11[3]); SB();
      const f32x4 G1 = mfma(X12, Z12, G1_k1); SB();
      const unsigned gp21_0 = pk(ga12[0], ga12[1]); SB();
      const unsigned gp21_1 = pk(ga12[2], ga12[3]); SB();
      const f32x4 ga20_k1 = mfma(WA21, X01, ga20_k0); SB();
      const unsigned gp31_0 = pk(ga13[0], ga13[1]); SB();
      const unsigned gp31_1 = pk(ga13[2], ga13[3]); SB();
      const f32x4 ga21_k1 = mfma(WA21, Z11, ga21_k0); SB();
      const f32x4 Sm1_k0 = mfma(ONES, Z10, Z4); SB();
      const f32x4 ga22_k1 = mfma(WA21, Z21, ga22_k0); SB();
      const float Gm0_j1 = diag1 ? G1[0] : G0[0]; SB();
      const float Gm1_j1 = diag1 ? G1[1] : G0[1]; SB();
      const f32x4 ga23_k1 = mfma(WA21, Z31, ga23_k0); SB();
      const float Gm2_j1 = diag1 ? G1[2] : G0[2]; SB();
      const float Gm3_j1 = diag1 ? G1[3] : G0[3]; SB();
      const f32x4 Sm1_k1 = mfma(ONES, Z11, Sm1_k0); SB();
      const f32x4 ga20 = mfma(WA22, X02, ga20_k1); SB();
      const f32x4 ga21 = mfma(WA22, Z12, ga21_k1); SB();
      auto X20 = X20_r; auto X21 = X21_r; auto X22 = X22_r; asm volatile("" : "+v"(X20), "+v"(X21), "+v"(X22)); SB();
      const f32x4 Sm1 = mfma(ONES, Z12, Sm1_k1); SB();
      auto WA30 = WA30_r; auto WA31 = WA31_r; auto WA32 = WA32_r; asm volatile("" : "+v"(WA30), "+v"(WA31), "+v"(WA32)); SB();
      const f32x4 ga22 = mfma(WA22, Z22, ga22_k1); SB();
      const f32x4 ga23 = mfma(WA22, Z32, ga23_k1); SB();
      const u32x4 WA40_r = WFRAG(0, 4, 0); SB();
      const u32x4 WA41_r = WFRAG(0, 4, 1); SB();
      const f32x4 G2_k0 = mfma(X20, Z20, Z4); SB();
      const u32x4 WA42_r = WFRAG(0, 4, 2); SB();
      const unsigned gp02_0 = pk(ga20[0], ga20[1]); SB();
      const f32x4 ga30_k0 = mfma(WA30, X00, Z4); SB();
      const float smm_j1 = diag1 ? Sm1[0] : Sm0[0]; SB();
      const unsigned gp02_1 = pk(ga20[2], ga20[3]); SB();
      const f32x4 ga31_k0 = mfma(WA30, Z10, Z4); SB();
      const unsigned gp12_0 = pk(ga21[0], ga21[1]); SB();
      const unsigned gp12_1 = pk(ga21[2], ga21[3]); SB();
      const f32x4 ga32_k0 = mfma(WA30, Z20, Z4); SB();
      const unsigned gp22_0 = pk(ga22[0], ga22[1]); SB();
      const unsigned gp22_1 = pk(ga22[2], ga22[3]); SB();
      const f32x4 ga33_k0 = mfma(WA30, Z30, Z4); SB();
      const unsigned gp32_0 = pk(ga23[0], ga23[1]); SB();
      const unsigned gp32_1 = pk(ga23[2], ga23[3]); SB();
      const f32x4 G2_k1 = mfma(X21, Z21, G2_k0); SB();
      const f32x4 ga30_k1 = mfma(WA31, X01, ga30_k0); SB();
      const f32x4 ga31_k1 = mfma(WA31, Z11, ga31_k0); SB();
      const f32x4 ga32_k1 = mfma(WA31, Z21, ga32_k0); SB();
      const f32x4 ga33_k1 = mfma(WA31, Z31, ga33_k0); SB();
      const f32x4 G2 = mfma(X22, Z22, G2_k1); SB();
      const f32x4 ga30 = mfma(WA32, X02, ga30_k1); SB();
      const f32x4 ga31 = mfma(WA32, Z12, ga31_k1); SB();
      auto WA40 = WA40_r; auto WA41 = WA41_r; auto WA42 = WA42_r; asm volatile("" : "+v"(WA40), "+v"(WA41), "+v"(WA42)); SB();
      const f32x4 ga32 = mfma(WA32, Z22, ga32_k1); SB();
      const u32x4 X30_r = lds_ld<u32x4>(raS + 0); SB();
      const u32x4 X31_r = lds_ld<u32x4>(raS + 64); SB();
      const f32x4 ga33 = mfma(WA32, Z32, ga33_k1); SB();
      const u32x4 X32_r = lds_ld<u32x4>(raS + 128); SB();
      const u32x4 WA50_r = WFRAG(0, 5, 0); SB();
      const f32x4 Sm2_k0 = mfma(ONES, Z20, Z4); SB();
      const u32x4 WA51_r = WFRAG(0, 5, 1); SB();
      const unsigned gp03_0 = pk(ga30[0], ga30[1]); SB();
      const f32x4 ga40_k0 = mfma(WA40, X00, Z4); SB();
      const u32x4 WA52_r = WFRAG(0, 5, 2); SB();
      const unsigned gp03_1 = pk(ga30[2], ga30[3]); SB();
      const f32x4 ga41_k0 = mfma(WA40, Z10, Z4); SB();
      const unsigned gp13_0 = pk(ga31[0], ga31[1]); SB();
      const unsigned gp13_1 = pk(ga31[2], ga31[3]); SB();
      const f32x4 ga42_k0 = mfma(WA40, Z20, Z4); SB();
      const unsigned gp23_0 = pk(ga32[0], ga32[1]); SB();
      const unsigned gp23_1 = pk(ga32[2], ga32[3]); SB();
      const f32x4 ga43_k0 = mfma(WA40, Z30, Z4); SB();
      const unsigned gp33_0 = pk(ga33[0], ga33[1]); SB();
      const unsigned gp33_1 = pk(ga33[2], ga33[3]); SB();
      const f32x4 Sm2_k1 = mfma(ONES, Z21, Sm2_k0); SB();
      const float Gm0_j2 = diag2 ? G2[0] : Gm0_j1; SB();
      const float Gm1_j2 = diag2 ? G2[1] : Gm1_j1; SB();
      const f32x4 ga40_k1 = mfma(WA41, X01, ga40_k0); SB();
      const float Gm2_j2 = diag2 ? G2[2] : Gm2_j1; SB();
      const float Gm3_j2 = diag2 ? G2[3] : Gm3_j1; SB();
      const f32x4 ga41_k1 = mfma(WA41, Z11, ga41_k0); SB();
      const f32x4 ga42_k1 = mfma(WA41, Z21, ga42_k0); SB();
      const f32x4 ga43_k1 = mfma(WA41, Z31, ga43_k0); SB();
      const f32x4 Sm2 = mfma(ONES, Z22, Sm2_k1); SB();
      const f32x4 ga40 = mfma(WA42, X02, ga40_k1); SB();
      auto X30 = X30_r; auto X31 = X31_r; auto X32 = X32_r; asm volatile("" : "+v"(X30), "+v"(X31), "+v"(X32)); SB();
      const f32x4 ga41 = mfma(WA42, Z12, ga41_k1); SB();
      auto WA50 = WA50_r; auto WA51 = WA51_r; auto WA52 = WA52_r; asm volatile("" : "+v"(WA50), "+v"(WA51), "+v"(WA52)); SB();
      const f32x4 ga42 = mfma(WA42, Z22, ga42_k1); SB();
      const f32x4 ga43 = mfma(WA42, Z32, ga43_k1); SB();
      const f32x4 G3_k0 = mfma(X30, Z30, Z4); SB();
      const f32x4 ga50_k0 = mfma(WA50, X00, Z4); SB();
      const u32x4 WB00_r = WFRAG(1, 0, 0); SB();
      const u32x4 WB01_r = WFRAG(1, 0, 1); SB();
      const f32x4 ga51_k0 = mfma(WA50, Z10, Z4); SB();
      const u32x4 WB02_r = WFRAG(1, 0, 2); SB();
      const float smm_j2 = diag2 ? Sm2[0] : smm_j1; SB();
      const f32x4 G3_k1 = mfma(X31, Z31, G3_k0); SB();
      const unsigned gp04_0 = pk(ga40[0], ga40[1]); SB();
      const unsigned gp04_1 = pk(ga40[2], ga40[3]); SB();
      const f32x4 ga52_k0 = mfma(WA50, Z20, Z4); SB();
      const unsigned gp14_0 = pk(ga41[0], ga41[1]); SB();
      const unsigned gp14_1 = pk(ga41[2], ga41[3]); SB();
      const f32x4 ga53_k0 = mfma(WA50, Z30, Z4); SB();
      const unsigned gp24_0 = pk(ga42[0], ga42[1]); SB();
      const unsigned gp24_1 = pk(ga42[2], ga42[3]); SB();
      const f32x4 G3 = mfma(X32, Z32, G3_k1); SB();
      const unsigned gp34_0 = pk(ga43[0], ga43[1]); SB();
      const unsigned gp34_1 = pk(ga43[2], ga43[3]); SB();
      const f32x4 ga50_k1 = mfma(WA51, X01, ga50_k0); SB();
      const f32x4 ga51_k1 = mfma(WA51, Z11, ga51_k0); SB();
      const f32x4 Sm3_k0 = mfma(ONES, Z30, Z4); SB();
      const f32x4 ga52_k1 = mfma(WA51, Z21, ga52_k0); SB();
      const f32x4 ga53_k1 = mfma(WA51, Z31, ga53_k0); SB();
      const f32x4 Sm3_k1 = mfma(ONES, Z31, Sm3_k0); SB();
      const float Gm0 = diag3 ? G3[0] : Gm0_j2; SB();
      const float Gm1 = diag3 ? G3[1] : Gm1_j2; SB();
      const f32x4 ga50 = mfma(WA52, X02, ga50_k1); SB();
      const float Gm2 = diag3 ? G3[2] : Gm2_j2; SB();
      const float Gm3 = diag3 ? G3[3] : Gm3_j2; SB();
      const f32x4 ga51 = mfma(WA52, Z12, ga51_k1); SB();
      auto WB00 = WB00_r; auto WB01 = WB01_r; auto WB02 = WB02_r; asm volatile("" : "+v"(WB00), "+v"(WB01), "+v"(WB02)); SB();
      const float r_a = t_odd ? Gm1 : Gm0; SB();
      const f32x4 Sm3 = mfma(ONES, Z32, Sm3_k1); SB();
      const float r_b = t_odd ? Gm3 : Gm2; SB();
      const float r_q = t_hi ? r_b : r_a; SB();
      const f32x4 ga52 = mfma(WA52, Z22, ga52_k1); SB();
      const unsigned gp05_0 = pk(ga50[0], ga50[1]); SB();
      const unsigned gp05_1 = pk(ga50[2], ga50[3]); SB();
      const f32x4 ga53 = mfma(WA52, Z32, ga53_k1); SB();
      const unsigned gp15_0 = pk(ga51[0], ga51[1]); SB();
      const unsigned gp15_1 = pk(ga51[2], ga51[3]); SB();
      const f32x4 ua00_k0 = mfma(X00, WB00, Z4); SB();
      const float smm = diag3 ? Sm3[0] : smm_j2; SB();
      KEEP(Sm0); KEEP(Sm1); KEEP(Sm2); KEEP(Sm3);
      const float r_s = smm * smm; SB();
      const f32x4 ua01_k0 = mfma(X10, WB00, Z4); SB();
      const unsigned gp25_0 = pk(ga52[0], ga52[1]); SB();
      const unsigned gp25_1 = pk(ga52[2], ga52[3]); SB();
      const f32x4 ua02_k0 = mfma(X20, WB00, Z4); SB();
      const unsigned gp35_0 = pk(ga53[0], ga53[1]); SB();
      const unsigned gp35_1 = pk(ga53[2], ga53[3]); SB();
      const f32x4 ua03_k0 = mfma(X30, WB00, Z4); SB();
      const float r_d = fmaf(r_q, 96.f, -r_s); SB();
      const float r_w = max2(r_d, 0.f); SB();
      const f32x4 ua00_k1 = mfma(X01, WB01, ua00_k0); SB();
      const float r_x = r_w + EPS96; SB();
      const float r_k = __builtin_amdgcn_fmed3f(r_w * 1e30f, 0.f, 1.f); SB();
      const f32x4 ua01_k1 = mfma(X11, WB01, ua01_k0); SB();
      const float r_y = __builtin_amdgcn_rsqf(r_x); SB();
      const f32x4 ua02_k1 = mfma(X21, WB01, ua02_k0); SB();
      const float r = fmaf(r_y, r_k, 1e-30f); SB();
      const f32x4 ua03_k1 = mfma(X31, WB01, ua03_k0); SB();
      const u32x4 WB10_r = WFRAG(1, 1, 0); SB();
      const u32x4 WB11_r = WFRAG(1, 1, 1); SB();
      const f32x4 ua00 = mfma(X02, WB02, ua00_k1); SB();
      const u32x4 WB12_r = WFRAG(1, 1, 2); SB();
      const f32x4 ua01 = mfma(X12, WB02, ua01_k1); SB();
      const f32x4 ua02 = mfma(X22, WB02, ua02_k1); SB();
      const f32x4 ua03 = mfma(X32, WB02, ua03_k1); SB();
      const f32x4 S0_k0 = mfma(X00, u32x4{gp00_0, gp00_1, gp01_0, gp01_1}, Z4); SB();
      const f32x4 S1_k0 = mfma(X10, u32x4{gp10_0, gp10_1, gp11_0, gp11_1}, Z4); SB();
      const f32x4 S2_k0 = mfma(X20, u32x4{gp20_0, gp20_1, gp21_0, gp21_1}, Z4); SB();
      const f32x4 S3_k0 = mfma(X30, u32x4{gp30_0, gp30_1, gp31_0, gp31_1}, Z4); SB();
      const unsigned up00_0 = pk(ua00[0], ua00[1]); SB();
      const unsigned up00_1 = pk(ua00[2], ua00[3]); SB();
      const f32x4 S0_k1 = mfma(X01, u32x4{gp02_0, gp02_1, gp03_0, gp03_1}, S0_k0); SB();
      const unsigned up10_0 = pk(ua01[0], ua01[1]); SB();
      const unsigned up10_1 = pk(ua01[2], ua01[3]); SB();
      const f32x4 S1_k1 = mfma(X11, u32x4{gp12_0, gp12_1, gp13_0, gp13_1}, S1_k0); SB();
      const unsigned up20_0 = pk(ua02[0], ua02[1]); SB();
      const unsigned up20_1 = pk(ua02[2], ua02[3]); SB();
      const f32x4 S2_k1 = mfma(X21, u32x4{gp22_0, gp22_1, gp23_0, gp23_1}, S2_k0); SB();
      const unsigned up30_0 = pk(ua03[0], ua03[1]); SB();
      const unsigned up30_1 = pk(ua03[2], ua03[3]); SB();
      const f32x4 S3_k1 = mfma(X31, u32x4{gp32_0, gp32_1, gp33_0, gp33_1}, S3_k0); SB();
      const u32x4 WB20_r = WFRAG(1, 2, 0); SB();
      const u32x4 WB21_r = WFRAG(1, 2, 1); SB();
      const f32x4 S0 = mfma(X02, u32x4{gp04_0, gp04_1, gp05_0, gp05_1}, S0_k1); SB();
      auto WB10 = WB10_r; auto WB11 = WB11_r; auto WB12 = WB12_r; asm volatile("" : "+v"(WB10), "+v"(WB11), "+v"(WB12)); SB();
      const u32x4 WB22_r = WFRAG(1, 2, 2); SB();
      const f32x4 S1 = mfma(X12, u32x4{gp14_0, gp14_1, gp15_0, gp15_1}, S1_k1); SB();
      const f32x4 S2 = mfma(X22, u32x4{gp24_0, gp24_1, gp25_0, gp25_1}, S2_k1); SB();
      const f32x4 S3 = mfma(X32, u32x4{gp34_0, gp34_1, gp35_0, gp35_1}, S3_k1); SB();
      const f32x4 ua10_k0 = mfma(X00, WB10, Z4); SB();
      const f32x4 ua11_k0 = mfma(X10, WB10, Z4); SB();
      const f32x4 ua12_k0 = mfma(X20, WB10, Z4); SB();
      const f32x4 ua13_k0 = mfma(X30, WB10, Z4); SB();
      const f32x4 ua10_k1 = mfma(X01, WB11, ua10_k0); SB();
      const f32x4 ua11_k1 = mfma(X11, WB11, ua11_k0); SB();
      const float Sx0_j1 = diag1 ? S1[0] : S0[0]; SB();
      const float Sx1_j1 = diag1 ? S1[1] : S0[1]; SB();
      const f32x4 ua12_k1 = mfma(X21, WB11, ua12_k0); SB();
      const float Sx2_j1 = diag1 ? S1[2] : S0[2]; SB();
      const float Sx0_j2 = diag2 ? S2[0] : Sx0_j1; SB();
      const f32x4 ua13_k1 = mfma(X31, WB11, ua13_k0); SB();
      const u32x4 WB30_r = WFRAG(1, 3, 0); SB();
      const float Sx1_j2 = diag2 ? S2[1] : Sx1_j1; SB();
      const f32x4 ua10 = mfma(X02, WB12, ua10_k1); SB();
      auto WB20 = WB20_r; auto WB21 = WB21_r; auto WB22 = WB22_r; asm volatile("" : "+v"(WB20), "+v"(WB21), "+v"(WB22)); SB();
      const float Sx2_j2 = diag2 ? S2[2] : Sx2_j1; SB();
      const f32x4 ua11 = mfma(X12, WB12, ua11_k1); SB();
      const u32x4 WB31_r = WFRAG(1, 3, 1); SB();
      const float Sx3_j1 = diag1 ? S1[3] : S0[3]; SB();
      const f32x4 ua12 = mfma(X22, WB12, ua12_k1); SB();
      const u32x4 WB32_r = WFRAG(1, 3, 2); SB();
      const float Sx0 = diag3 ? S3[0] : Sx0_j2; SB();
      const f32x4 ua13 = mfma(X32, WB12, ua13_k1); SB();
      const float Sx1 = diag3 ? S3[1] : Sx1_j2; SB();
      const float Sx2 = diag3 ? S3[2] : Sx2_j2; SB();
      const f32x4 ua20_k0 = mfma(X00, WB20, Z4); SB();
      const float Sx3_j2 = diag2 ? S2[3] : Sx3_j1; SB();
      const float Sx3 = diag3 ? S3[3] : Sx3_j2; SB();
      const f32x4 ua21_k0 = mfma(X10, WB20, Z4); SB();
      const float Pm_t0 = Sx0 * dpp<0x00>(r); SB();
      const float Pm_t1 = Sx1 * dpp<0x55>(r); SB();
      const f32x4 ua22_k0 = mfma(X20, WB20, Z4); SB();
      const float Pm_t2 = Sx2 * dpp<0xAA>(r); SB();
      const float Pm_t3 = Sx3 * dpp<0xFF>(r); SB();
      const f32x4 ua23_k0 = mfma(X30, WB20, Z4); SB();
      const float Pm_m0 = max3(Pm_t0, Pm_t1, Pm_t2); SB();
      const float Pm_m1 = max2(Pm_m0, Pm_t3); SB();
      const f32x4 ua20_k1 = mfma(X01, WB21, ua20_k0); SB();
      const float Pm_mn = -Pm_m1 * r; SB();
      const float Pm_z0 = fmaf(Pm_t0, r, Pm_mn); SB();
      const f32x4 ua21_k1 = mfma(X11, WB21, ua21_k0); SB();
      const float Pm_z1 = fmaf(Pm_t1, r, Pm_mn); SB();
      const float Pm_z2 = fmaf(Pm_t2, r, Pm_mn); SB();
      const f32x4 ua22_k1 = mfma(X21, WB21, ua22_k0); SB();
      const float Pm_z3 = fmaf(Pm_t3, r, Pm_mn); SB();
      const float Pm_r2 = opaque(r);
      const float Pm_e0 = exp2_(Pm_z0); SB();
      const f32x4 ua23_k1 = mfma(X31, WB21, ua23_k0); SB();
      const u32x4 WB40_r = WFRAG(1, 4, 0); SB();
      const float Pm_e1 = exp2_(Pm_z1); SB();
      const f32x4 ua20 = mfma(X02, WB22, ua20_k1); SB();
      auto WB30 = WB30_r; auto WB31 = WB31_r; auto WB32 = WB32_r; asm volatile("" : "+v"(WB30), "+v"(WB31), "+v"(WB32)); SB();
      const float Pm_e2 = exp2_(Pm_z2); SB();
      const f32x4 ua21 = mfma(X12, WB22, ua21_k1); SB();
      const u32x4 WB41_r = WFRAG(1, 4, 1); SB();
      const float Pm_e3 = exp2_(Pm_z3); SB();
      const f32x4 ua22 = mfma(X22, WB22, ua22_k1); SB();
      const u32x4 WB42_r = WFRAG(1, 4, 2); SB();
      const float Pm_l0 = Pm_e0 + Pm_e1; SB();
      const f32x4 ua23 = mfma(X32, WB22, ua23_k1); SB();
      const float Pm_l1 = Pm_e2 + Pm_e3; SB();
      const float Pm_l = Pm_l0 + Pm_l1; SB();
      const f32x4 ua30_k0 = mfma(X00, WB30, Z4); SB();
      const float Pm_i = __builtin_amdgcn_rcpf(Pm_l); SB();
      const f32x4 ua31_k0 = mfma(X10, WB30, Z4); SB();
      const float Pm_im = Pm_i * 96.f; SB();
      const float Pm_w0 = dpp<0x00>(Pm_r2) * Pm_im; SB();
      const f32x4 ua32_k0 = mfma(X20, WB30, Z4); SB();
      const float Pm_w1 = dpp<0x55>(Pm_r2) * Pm_im; SB();
      const float Pm_w2 = dpp<0xAA>(Pm_r2) * Pm_im; SB();
      const f32x4 ua33_k0 = mfma(X30, WB30, Z4); SB();
      const float Pm_w3 = dpp<0xFF>(Pm_r2) * Pm_im; SB();
      const float Pm_p0 = Pm_e0 * Pm_w0; SB();
      const f32x4 ua30_k1 = mfma(X01, WB31, ua30_k0); SB();
      const float Pm_p1 = Pm_e1 * Pm_w1; SB();
      const float Pm_p2 = Pm_e2 * Pm_w2; SB();
      const f32x4 ua31_k1 = mfma(X11, WB31, ua31_k0); SB();
      const float Pm_p3 = Pm_e3 * Pm_w3; SB();
      const unsigned Pm_u0 = pk(Pm_p0, Pm_p1); SB();
      const f32x4 ua32_k1 = mfma(X21, WB31, ua32_k0); SB();
      const unsigned Pm_u1 = pk(Pm_p2, Pm_p3); SB();
      const unsigned P0_0 = diag0 ? Pm_u0 : 0u; SB();
      const f32x4 ua33_k1 = mfma(X31, WB31, ua33_k0); SB();
      const u32x4 WB50_r = WFRAG(1, 5, 0); SB();
      const unsigned id0_0 = diag0 ? IDR0 : 0u; SB();
      const f32x4 ua30 = mfma(X02, WB32, ua30_k1); SB();
      auto WB40 = WB40_r; auto WB41 = WB41_r; auto WB42 = WB42_r; asm volatile("" : "+v"(WB40), "+v"(WB41), "+v"(WB42)); SB();
      const unsigned P0_1 = diag0 ? Pm_u1 : 0u; SB();
      const f32x4 ua31 = mfma(X12, WB32, ua31_k1); SB();
      const u32x4 WB51_r = WFRAG(1, 5, 1); SB();
      const unsigned id0_1 = diag0 ? IDR1 : 0u; SB();
      const f32x4 ua32 = mfma(X22, WB32, ua32_k1); SB();
      const u32x4 WB52_r = WFRAG(1, 5, 2); SB();
      const unsigned P1_0 = diag1 ? Pm_u0 : 0u; SB();
      const f32x4 ua33 = mfma(X32, WB32, ua33_k1); SB();
      const unsigned id1_0 = diag1 ? IDR0 : 0u; SB();
      const unsigned P1_1 = diag1 ? Pm_u1 : 0u; SB();
      const f32x4 ua40_k0 = mfma(X00, WB40, Z4); SB();
      const unsigned id1_1 = diag1 ? IDR1 : 0u; SB();
      const unsigned P2_0 = diag2 ? Pm_u0 : 0u; SB();
      const f32x4 ua41_k0 = mfma(X10, WB40, Z4); SB();
      const unsigned id2_0 = diag2 ? IDR0 : 0u; SB();
      const unsigned P2_1 = diag2 ? Pm_u1 : 0u; SB();
      const f32x4 ua42_k0 = mfma(X20, WB40, Z4); SB();
      const unsigned id2_1 = diag2 ? IDR1 : 0u; SB();
      const unsigned P3_0 = diag3 ? Pm_u0 : 0u; SB();
      const f32x4 ua43_k0 = mfma(X30, WB40, Z4); SB();
      const unsigned id3_0 = diag3 ? IDR0 : 0u; SB();
      const unsigned P3_1 = diag3 ? Pm_u1 : 0u; SB();
      const f32x4 ua40_k1 = mfma(X01, WB41, ua40_k0); SB();
      const unsigned id3_1 = diag3 ? IDR1 : 0u; SB();
      const unsigned up01_0 = pk(ua10[0], ua10[1]); SB();
      const f32x4 ua41_k1 = mfma(X11, WB41, ua41_k0); SB();
      const unsigned up01_1 = pk(ua10[2], ua10[3]); SB();
      const unsigned up11_0 = pk(ua11[0], ua11[1]); SB();
      const f32x4 ua42_k1 = mfma(X21, WB41, ua42_k0); SB();
      const unsigned up11_1 = pk(ua11[2], ua11[3]); SB();
      const unsigned up21_0 = pk(ua12[0], ua12[1]); SB();
      const f32x4 ua43_k1 = mfma(X31, WB41, ua43_k0); SB();
      const u32x2 XT00_r = lds_tr(TA(0) + 0); SB();
      const unsigned up21_1 = pk(ua12[2], ua12[3]); SB();
      const f32x4 ua40 = mfma(X02, WB42, ua40_k1); SB();
      auto WB50 = WB50_r; auto WB51 = WB51_r; auto WB52 = WB52_r; asm volatile("" : "+v"(WB50), "+v"(WB51), "+v"(WB52)); SB();
      const unsigned up31_0 = pk(ua13[0], ua13[1]); SB();
      const f32x4 ua41 = mfma(X12, WB42, ua41_k1); SB();
      const u32x2 XT10_r = lds_tr(TA(1) + 0); SB();
      const unsigned up31_1 = pk(ua13[2], ua13[3]); SB();
      const f32x4 ua42 = mfma(X22, WB42, ua42_k1); SB();
      const u32x2 XT20_r = lds_tr(TA(2) + 0); SB();
      const unsigned up02_0 = pk(ua20[0], ua20[1]); SB();
      const f32x4 ua43 = mfma(X32, WB42, ua43_k1); SB();
      const u32x2 XT30_r = lds_tr(taS + 0); SB();
      const unsigned up02_1 = pk(ua20[2], ua20[3]); SB();
      const f32x4 ua50_k0 = mfma(X00, WB50, Z4); SB();
      const u32x2 XT01_r = lds_tr(TA(0) + 8); SB();
      const unsigned up12_0 = pk(ua21[0], ua21[1]); SB();
      const f32x4 ua51_k0 = mfma(X10, WB50, Z4); SB();
      const u32x2 XT11_r = lds_tr(TA(1) + 8); SB();
      const unsigned up12_1 = pk(ua21[2], ua21[3]); SB();
      const f32x4 ua52_k0 = mfma(X20, WB50, Z4); SB();
      const u32x2 XT21_r = lds_tr(TA(2) + 8); SB();
      const unsigned up22_0 = pk(ua22[0], ua22[1]); SB();
      const f32x4 ua53_k0 = mfma(X30, WB50, Z4); SB();
      const u32x2 XT31_r = lds_tr(taS + 8); SB();
      const unsigned up22_1 = pk(ua22[2], ua22[3]); SB();
      const f32x4 ua50_k1 = mfma(X01, WB51, ua50_k0); SB();
      const u32x2 XT02_r = lds_tr(TA(0) + 64); SB();
      const unsigned up32_0 = pk(ua23[0], ua23[1]); SB();
      const f32x4 ua51_k1 = mfma(X11, WB51, ua51_k0); SB();
      const u32x2 XT12_r = lds_tr(TA(1) + 64); SB();
      const unsigned up32_1 = pk(ua23[2], ua23[3]); SB();
      const f32x4 ua52_k1 = mfma(X21, WB51, ua52_k0); SB();
      const u32x2 XT22_r = lds_tr(TA(2) + 64); SB();
      const unsigned up03_0 = pk(ua30[0], ua30[1]); SB();
      const f32x4 ua53_k1 = mfma(X31, WB51, ua53_k0); SB();
      const u32x2 XT32_r = lds_tr(taS + 64); SB();
      const unsigned up03_1 = pk(ua30[2], ua30[3]); SB();
      const f32x4 ua50 = mfma(X02, WB52, ua50_k1); SB();
      const u32x2 XT03_r = lds_tr(TA(0) + 72); SB();
      const unsigned up13_0 = pk(ua31[0], ua31[1]); SB();
      const f32x4 ua51 = mfma(X12, WB52, ua51_k1); SB();
      auto XT00 = XT00_r; auto XT10 = XT10_r; auto XT20 = XT20_r; auto XT30 = XT30_r; asm volatile("" : "+v"(XT00), "+v"(XT10), "+v"(XT20), "+v"(XT30)); SB();
      const unsigned up13_1 = pk(ua31[2], ua31[3]); SB();
      const f32x4 ua52 = mfma(X22, WB52, ua52_k1); SB();
      const u32x2 XT13_r = lds_tr(TA(1) + 72); SB();
      const unsigned up23_0 = pk(ua32[0], ua32[1]); SB();
      const f32x4 ua53 = mfma(X32, WB52, ua53_k1); SB();
      const u32x2 XT23_r = lds_tr(TA(2) + 72); SB();
      const unsigned up23_1 = pk(ua32[2], ua32[3]); SB();
      const f32x4 o00 = mfma(u32x4{up00_0, up00_1, XT00[0], XT00[1]}, u32x4{P0_0, P0_1, id0_0, id0_1}, Z4); SB();
      const u32x2 XT33_r = lds_tr(taS + 72); SB();
      const unsigned up33_0 = pk(ua33[0], ua33[1]); SB();
      const f32x4 o01 = mfma(u32x4{up10_0, up10_1, XT10[0], XT10[1]}, u32x4{P1_0, P1_1, id1_0, id1_1}, Z4); SB();
      auto XT01 = XT01_r; auto XT11 = XT11_r; auto XT21 = XT21_r; auto XT31 = XT31_r; asm volatile("" : "+v"(XT01), "+v"(XT11), "+v"(XT21), "+v"(XT31)); SB();
      const unsigned up33_1 = pk(ua33[2], ua33[3]); SB();
      const f32x4 o02 = mfma(u32x4{up20_0, up20_1, XT20[0], XT20[1]}, u32x4{P2_0, P2_1, id2_0, id2_1}, Z4); SB();
      const unsigned up04_0 = pk(ua40[0], ua40[1]); SB();
      const unsigned up04_1 = pk(ua40[2], ua40[3]); SB();
      const f32x4 o03 = mfma(u32x4{up30_0, up30_1, XT30[0], XT30[1]}, u32x4{P3_0, P3_1, id3_0, id3_1}, Z4); SB();
      const u32x2 XT04_r = lds_tr(TA(0) + 128); SB();
      const unsigned x100_0 = pk(o00[0], o00[1]); SB();
      const f32x4 o10 = mfma(u32x4{up01_0, up01_1, XT01[0], XT01[1]}, u32x4{P0_0, P0_1, id0_0, id0_1}, Z4); SB();
      const u32x2 XT14_r = lds_tr(TA(1) + 128); SB();
      const unsigned x100_1 = pk(o00[2], o00[3]); SB();
      const f32x4 o11 = mfma(u32x4{up11_0, up11_1, XT11[0], XT11[1]}, u32x4{P1_0, P1_1, id1_0, id1_1}, Z4); SB();
      auto XT02 = XT02_r; auto XT12 = XT12_r; auto XT22 = XT22_r; auto XT32 = XT32_r; asm volatile("" : "+v"(XT02), "+v"(XT12), "+v"(XT22), "+v"(XT32)); SB();
      const unsigned x110_0 = pk(o01[0], o01[1]); SB();
      const f32x4 o12 = mfma(u32x4{up21_0, up21_1, XT21[0], XT21[1]}, u32x4{P2_0, P2_1, id2_0, id2_1}, Z4); SB();
      const u32x2 XT24_r = lds_tr(TA(2) + 128); SB();
      const unsigned x110_1 = pk(o01[2], o01[3]); SB();
      const f32x4 o13 = mfma(u32x4{up31_0, up31_1, XT31[0], XT31[1]}, u32x4{P3_0, P3_1, id3_0, id3_1}, Z4); SB();
      const u32x2 XT34_r = lds_tr(taS + 128); SB();
      const unsigned x101_0 = pk(o10[0], o10[1]); SB();
      const f32x4 o20 = mfma(u32x4{up02_0, up02_1, XT02[0], XT02[1]}, u32x4{P0_0, P0_1, id0_0, id0_1}, Z4); SB();
      const unsigned x101_1 = pk(o10[2], o10[3]); SB();
      const unsigned x111_0 = pk(o11[0], o11[1]); SB();
      const f32x4 o21 = mfma(u32x4{up12_0, up12_1, XT12[0], XT12[1]}, u32x4{P1_0, P1_1, id1_0, id1_1}, Z4); SB();
      const unsigned x111_1 = pk(o11[2], o11[3]); SB();
      const unsigned up14_0 = pk(ua41[0], ua41[1]); SB();
      const f32x4 o22 = mfma(u32x4{up22_0, up22_1, XT22[0], XT22[1]}, u32x4{P2_0, P2_1, id2_0, id2_1}, Z4); SB();
      auto XT03 = XT03_r; auto XT13 = XT13_r; auto XT23 = XT23_r; auto XT33 = XT33_r; asm volatile("" : "+v"(XT03), "+v"(XT13), "+v"(XT23), "+v"(XT33)); SB();
      const unsigned up14_1 = pk(ua41[2], ua41[3]); SB();
      const f32x4 o23 = mfma(u32x4{up32_0, up32_1, XT32[0], XT32[1]}, u32x4{P3_0, P3_1, id3_0, id3_1}, Z4); SB();
      const u32x2 XT05_r = lds_tr(TA(0) + 136); SB();
      const unsigned up24_0 = pk(ua42[0], ua42[1]); SB();
      const f32x4 o30 = mfma(u32x4{up03_0, up03_1, XT03[0], XT03[1]}, u32x4{P0_0, P0_1, id0_0, id0_1}, Z4); SB();
      const u32x2 XT15_r = lds_tr(TA(1) + 136); SB();
      const unsigned up24_1 = pk(ua42[2], ua42[3]); SB();
      const f32x4 o31 = mfma(u32x4{up13_0, up13_1, XT13[0], XT13[1]}, u32x4{P1_0, P1_1, id1_0, id1_1}, Z4); SB();
      const u32x2 XT25_r = lds_tr(TA(2) + 136); SB();
      const unsigned up34_0 = pk(ua43[0], ua43[1]); SB();
      const f32x4 o32 = mfma(u32x4{up23_0, up23_1, XT23[0], XT23[1]}, u32x4{P2_0, P2_1, id2_0, id2_1}, Z4); SB();
      const u32x2 XT35_r = lds_tr(taS + 136); SB();
      const unsigned up34_1 = pk(ua43[2], ua43[3]); SB();
      const f32x4 o33 = mfma(u32x4{up33_0, up33_1, XT33[0], XT33[1]}, u32x4{P3_0, P3_1, id3_0, id3_1}, Z4); SB();
      lds_st<u32x4>(raE + 0, u32x4{x100_0, x100_1, x101_0, x101_1}); SB();
      const unsigned x120_0 = pk(o02[0], o02[1]); SB();
      const f32x4 G20_k0 = mfma(u32x4{x100_0, x100_1, x101_0, x101_1}, u32x4{x100_0, x100_1, x101_0, x101_1}, Z4); SB();
      lds_st<u32x4>(rb1 + 0, u32x4{x110_0, x110_1, x111_0, x111_1}); SB();
      const unsigned x120_1 = pk(o02[2], o02[3]); SB();
      const f32x4 S20_k0 = mfma(ONES, u32x4{x100_0, x100_1, x101_0, x101_1}, Z4); SB();
      auto XT04 = XT04_r; auto XT14 = XT14_r; auto XT24 = XT24_r; auto XT34 = XT34_r; asm volatile("" : "+v"(XT04), "+v"(XT14), "+v"(XT24), "+v"(XT34)); SB();
      const unsigned x121_0 = pk(o12[0], o12[1]); SB();
      const f32x4 G21_k0 = mfma(u32x4{x110_0, x110_1, x111_0, x111_1}, u32x4{x110_0, x110_1, x111_0, x111_1}, Z4); SB();
      const unsigned x121_1 = pk(o12[2], o12[3]); SB();
      const unsigned x130_0 = pk(o03[0], o03[1]); SB();
      const f32x4 S21_k0 = mfma(ONES, u32x4{x110_0, x110_1, x111_0, x111_1}, Z4); SB();
      lds_st<u32x4>(rb2 + 0, u32x4{x120_0, x120_1, x121_0, x121_1}); SB();
      const unsigned x130_1 = pk(o03[2], o03[3]); SB();
      const f32x4 o40 = mfma(u32x4{up04_0, up04_1, XT04[0], XT04[1]}, u32x4{P0_0, P0_1, id0_0, id0_1}, Z4); SB();
      const unsigned x131_0 = pk(o13[0], o13[1]); SB();
      const unsigned x131_1 = pk(o13[2], o13[3]); SB();
      const f32x4 o41 = mfma(u32x4{up14_0, up14_1, XT14[0], XT14[1]}, u32x4{P1_0, P1_1, id1_0, id1_1}, Z4); SB();
      lds_st<u32x4>(rb3 + 0, u32x4{x130_0, x130_1, x131_0, x131_1}); SB();
      const unsigned up05_0 = pk(ua50[0], ua50[1]); SB();
      const f32x4 o42 = mfma(u32x4{up24_0, up24_1, XT24[0], XT24[1]}, u32x4{P2_0, P2_1, id2_0, id2_1}, Z4); SB();
      const unsigned up05_1 = pk(ua50[2], ua50[3]); SB();
      const unsigned up15_0 = pk(ua51[0], ua51[1]); SB();
      const f32x4 o43 = mfma(u32x4{up34_0, up34_1, XT34[0], XT34[1]}, u32x4{P3_0, P3_1, id3_0, id3_1}, Z4); SB();
      const unsigned up15_1 = pk(ua51[2], ua51[3]); SB();
      const unsigned up25_0 = pk(ua52[0], ua52[1]); SB();
      const f32x4 G22_k0 = mfma(u32x4{x120_0, x120_1, x121_0, x121_1}, u32x4{x120_0, x120_1, x121_0, x121_1}, Z4); SB();
      auto XT05 = XT05_r; auto XT15 = XT15_r; auto XT25 = XT25_r; auto XT35 = XT35_r; asm volatile("" : "+v"(XT05), "+v"(XT15), "+v"(XT25), "+v"(XT35)); SB();
      const unsigned up25_1 = pk(ua52[2], ua52[3]); SB();
      const f32x4 S22_k0 = mfma(ONES, u32x4{x120_0, x120_1, x121_0, x121_1}, Z4); SB();
      const unsigned up35_0 = pk(ua53[0], ua53[1]); SB();
      const unsigned up35_1 = pk(ua53[2], ua53[3]); SB();
      const f32x4 G23_k0 = mfma(u32x4{x130_0, x130_1, x131_0, x131_1}, u32x4{x130_0, x130_1, x131_0, x131_1}, Z4); SB();
      const unsigned x102_0 = pk(o20[0], o20[1]); SB();
      const unsigned x102_1 = pk(o20[2], o20[3]); SB();
      const f32x4 S23_k0 = mfma(ONES, u32x4{x130_0, x130_1, x131_0, x131_1}, Z4); SB();
      const unsigned x103_0 = pk(o30[0], o30[1]); SB();
      const unsigned x103_1 = pk(o30[2], o30[3]); SB();
      const f32x4 o50 = mfma(u32x4{up05_0, up05_1, XT05[0], XT05[1]}, u32x4{P0_0, P0_1, id0_0, id0_1}, Z4); SB();
      lds_st<u32x4>(raE + 64, u32x4{x102_0, x102_1, x103_0, x103_1}); SB();
      const unsigned x112_0 = pk(o21[0], o21[1]); SB();
      const f32x4 o51 = mfma(u32x4{up15_0, up15_1, XT15[0], XT15[1]}, u32x4{P1_0, P1_1, id1_0, id1_1}, Z4); SB();
      const unsigned x112_1 = pk(o21[2], o21[3]); SB();
      const unsigned x113_0 = pk(o31[0], o31[1]); SB();
      const f32x4 o52 = mfma(u32x4{up25_0, up25_1, XT25[0], XT25[1]}, u32x4{P2_0, P2_1, id2_0, id2_1}, Z4); SB();
      const unsigned x113_1 = pk(o31[2], o31[3]); SB();
      const unsigned x122_0 = pk(o22[0], o22[1]); SB();
      const f32x4 o53 = mfma(u32x4{up35_0, up35_1, XT35[0], XT35[1]}, u32x4{P3_0, P3_1, id3_0, id3_1}, Z4); SB();
      lds_st<u32x4>(rb1 + 64, u32x4{x112_0, x112_1, x113_0, x113_1}); SB();
      const unsigned x122_1 = pk(o22[2], o22[3]); SB();
      const f32x4 G20_k1 = mfma(u32x4{x102_0, x102_1, x103_0, x103_1}, u32x4{x102_0, x102_1, x103_0, x103_1}, G20_k0); SB();
      const unsigned x123_0 = pk(o32[0], o32[1]); SB();
      const unsigned x123_1 = pk(o32[2], o32[3]); SB();
      const f32x4 S20_k1 = mfma(ONES, u32x4{x102_0, x102_1, x103_0, x103_1}, S20_k0); SB();
      lds_st<u32x4>(rb2 + 64, u32x4{x122_0, x122_1, x123_0, x123_1}); SB();
      const unsigned x132_0 = pk(o23[0], o23[1]); SB();
      const f32x4 G21_k1 = mfma(u32x4{x112_0, x112_1, x113_0, x113_1}, u32x4{x112_0, x112_1, x113_0, x113_1}, G21_k0); SB();
      const unsigned x132_1 = pk(o23[2], o23[3]); SB();
      const unsigned x133_0 = pk(o33[0], o33[1]); SB();
      const f32x4 S21_k1 = mfma(ONES, u32x4{x112_0, x112_1, x113_0, x113_1}, S21_k0); SB();
      const unsigned x133_1 = pk(o33[2], o33[3]); SB();
      const unsigned x104_0 = pk(o40[0], o40[1]); SB();
      const f32x4 G22_k1 = mfma(u32x4{x122_0, x122_1, x123_0, x123_1}, u32x4{x122_0, x122_1, x123_0, x123_1}, G22_k0); SB();
      lds_st<u32x4>(rb3 + 64, u32x4{x132_0, x132_1, x133_0, x133_1}); SB();
      const unsigned x104_1 = pk(o40[2], o40[3]); SB();
      const f32x4 S22_k1 = mfma(ONES, u32x4{x122_0, x122_1, x123_0, x123_1}, S22_k0); SB();
      const unsigned x105_0 = pk(o50[0], o50[1]); SB();
      const unsigned x105_1 = pk(o50[2], o50[3]); SB();
      const f32x4 G23_k1 = mfma(u32x4{x132_0, x132_1, x133_0, x133_1}, u32x4{x132_0, x132_1, x133_0, x133_1}, G23_k0); SB();
      lds_st<u32x4>(raE + 128, u32x4{x104_0, x104_1, x105_0, x105_1}); SB();
      const unsigned x114_0 = pk(o41[0], o41[1]); SB();
      const f32x4 S23_k1 = mfma(ONES, u32x4{x132_0, x132_1, x133_0, x133_1}, S23_k0); SB();
      const unsigned x114_1 = pk(o41[2], o41[3]); SB();
      const unsigned x115_0 = pk(o51[0], o51[1]); SB();
      const f32x4 G20 = mfma(u32x4{x104_0, x104_1, x105_0, x105_1}, u32x4{x104_0, x104_1, x105_0, x105_1}, G20_k1); SB();
      const unsigned x115_1 = pk(o51[2], o51[3]); SB();
      const unsigned x124_0 = pk(o42[0], o42[1]); SB();
      const f32x4 S20 = mfma(ONES, u32x4{x104_0, x104_1, x105_0, x105_1}, S20_k1); SB();
      lds_st<u32x4>(rb1 + 128, u32x4{x114_0, x114_1, x115_0, x115_1}); SB();
      const unsigned x124_1 = pk(o42[2], o42[3]); SB();
      const f32x4 G21 = mfma(u32x4{x114_0, x114_1, x115_0, x115_1}, u32x4{x114_0, x114_1, x115_0, x115_1}, G21_k1); SB();
      const unsigned x125_0 = pk(o52[0], o52[1]); SB();
      const unsigned x125_1 = pk(o52[2], o52[3]); SB();
      const f32x4 S21 = mfma(ONES, u32x4{x114_0, x114_1, x115_0, x115_1}, S21_k1); SB();
      lds_st<u32x4>(rb2 + 128, u32x4{x124_0, x124_1, x125_0, x125_1}); SB();
      const unsigned x134_0 = pk(o43[0], o43[1]); SB();
      const f32x4 G22 = mfma(u32x4{x124_0, x124_1, x125_0, x125_1}, u32x4{x124_0, x124_1, x125_0, x125_1}, G22_k1); SB();
      const unsigned x134_1 = pk(o43[2], o43[3]); SB();
      const unsigned x135_0 = pk(o53[0], o53[1]); SB();
      const f32x4 S22 = mfma(ONES, u32x4{x124_0, x124_1, x125_0, x125_1}, S22_k1); SB();
      const unsigned x135_1 = pk(o53[2], o53[3]); SB();
      const float q0_a = t_odd ? G20[1] : G20[0]; SB();
      const float q0_b = t_odd ? G20[3] : G20[2]; SB();
      const f32x4 G23 = mfma(u32x4{x134_0, x134_1, x135_0, x135_1}, u32x4{x134_0, x134_1, x135_0, x135_1}, G23_k1); SB();
      lds_st<u32x4>(rb3 + 128, u32x4{x134_0, x134_1, x135_0, x135_1}); SB();
      const float q1_a = t_odd ? G21[1] : G21[0]; SB();
      const f32x4 S23 = mfma(ONES, u32x4{x134_0, x134_1, x135_0, x135_1}, S23_k1); SB();
      const float q1_b = t_odd ? G21[3] : G21[2]; SB();
      const float q2_a = t_odd ? G22[1] : G22[0]; SB();
      const float q2_b = t_odd ? G22[3] : G22[2]; SB();
      const float q0_q = t_hi ? q0_b : q0_a; SB();
      const float q0_s = S20[0] * S20[0]; KEEP(S20); SB();
      const float q3_a = t_odd ? G23[1] : G23[0]; SB();
      const float q3_b = t_odd ? G23[3] : G23[2]; SB();
      const float q1_q = t_hi ? q1_b : q1_a; SB();
      const float q1_s = S21[0] * S21[0]; KEEP(S21); SB();
      const float q2_q = t_hi ? q2_b : q2_a; SB();
      const float q2_s = S22[0] * S22[0]; KEEP(S22); SB();
      const float q3_q = t_hi ? q3_b : q3_a; SB();
      const float q3_s = S23[0] * S23[0]; KEEP(S23); SB();
      const float q0_d = fmaf(q0_q, 96.f, -q0_s); SB();
      const float q1_d = fmaf(q1_q, 96.f, -q1_s); SB();
      const float q2_d = fmaf(q2_q, 96.f, -q2_s); SB();
      const float q3_d = fmaf(q3_q, 96.f, -q3_s); SB();
      const float q0_w = max2(q0_d, 0.f); SB();
      const float q1_w = max2(q1_d, 0.f); SB();
      const float q2_w = max2(q2_d, 0.f); SB();
      const float q3_w = max2(q3_d, 0.f); SB();
      const float q0_x = q0_w + EPS96; SB();
      const float q1_x = q1_w + EPS96; SB();
      const float q2_x = q2_w + EPS96; SB();
      const float q3_x = q3_w + EPS96; SB();
      const float q0_k = __builtin_amdgcn_fmed3f(q0_w * 1e30f, 0.f, 1.f); SB();
      const float q0_y = __builtin_amdgcn_rsqf(q0_x); SB();
      const float q1_k = __builtin_amdgcn_fmed3f(q1_w * 1e30f, 0.f, 1.f); SB();
      const float q1_y = __builtin_amdgcn_rsqf(q1_x); SB();
      const float q2_k = __builtin_amdgcn_fmed3f(q2_w * 1e30f, 0.f, 1.f); SB();
      const float q2_y = __builtin_amdgcn_rsqf(q2_x); SB();
      const float q3_k = __builtin_amdgcn_fmed3f(q3_w * 1e30f, 0.f, 1.f); SB();
      const float q3_y = __builtin_amdgcn_rsqf(q3_x); SB();
      const float q0 = fmaf(q0_y, q0_k, 1e-30f); SB();
      const float q1 = fmaf(q1_y, q1_k, 1e-30f); SB();
      const float q2 = fmaf(q2_y, q2_k, 1e-30f); SB();
      const float q3 = fmaf(q3_y, q3_k, 1e-30f); SB();
      const float qq0 = q0 * 96.f; SB();
      const float qq1 = q1 * 96.f; SB();
      const float qq2 = q2 * 96.f; SB();
      const float qq3 = q3 * 96.f; SB();
      lds_st<float>(r2a0, qq0); SB();
      lds_st<float>(r2a1, qq1); SB();
      lds_st<float>(r2a2, qq2); SB();
      lds_st<float>(r2a3, qq3); SB();
      flag_arrive(fB1); SB();
      flag_wait(lb + FLG + 48, 4); SB();
      const u32x4 VA00_r = WFRAG2(0, 0, 0); SB();
      const u32x4 VA01_r = WFRAG2(0, 0, 1); SB();
      const u32x4 VA02_r = WFRAG2(0, 0, 2); SB();
      const u32x4 VA10_r = WFRAG2(0, 1, 0); SB();
      const u32x4 VA11_r = WFRAG2(0, 1, 1); SB();
      const u32x4 VA12_r = WFRAG2(0, 1, 2); SB();
      const u32x4 VA20_r = WFRAG2(0, 2, 0); SB();
      const u32x4 VA21_r = WFRAG2(0, 2, 1); SB();
      const u32x4 VA22_r = WFRAG2(0, 2, 2); SB();
      const u32x4 VA30_r = WFRAG2(0, 3, 0); SB();
      const u32x4 VA31_r = WFRAG2(0, 3, 1); SB();
      const u32x4 VA32_r = WFRAG2(0, 3, 2); SB();
      const u32x4 VA40_r = WFRAG2(0, 4, 0); SB();
      const u32x4 VA41_r = WFRAG2(0, 4, 1); SB();
      const u32x4 VA42_r = WFRAG2(0, 4, 2); SB();
      const u32x4 VA50_r = WFRAG2(0, 5, 0); SB();
      const u32x4 VA51_r = WFRAG2(0, 5, 1); SB();
      const u32x4 VA52_r = WFRAG2(0, 5, 2); SB();
      const u32x4 VB00_r = WFRAG2(1, 0, 0); SB();
      const u32x4 VB01_r = WFRAG2(1, 0, 1); SB();
      const u32x4 VB02_r = WFRAG2(1, 0, 2); SB();
      const u32x4 VB10_r = WFRAG2(1, 1, 0); SB();
      const u32x4 VB11_r = WFRAG2(1, 1, 1); SB();
      const u32x4 VB12_r = WFRAG2(1, 1, 2); SB();
      const u32x4 VB20_r = WFRAG2(1, 2, 0); SB();
      const u32x4 VB21_r = WFRAG2(1, 2, 1); SB();
      const u32x4 VB22_r = WFRAG2(1, 2, 2); SB();
      const u32x4 VB30_r = WFRAG2(1, 3, 0); SB();
      const u32x4 VB31_r = WFRAG2(1, 3, 1); SB();
      const u32x4 VB32_r = WFRAG2(1, 3, 2); SB();
      auto VA00 = VA00_r; auto VA01 = VA01_r; auto VA02 = VA02_r; asm volatile("" : "+v"(VA00), "+v"(VA01), "+v"(VA02)); SB();
      const f32x4 VAa0_k0 = mfma(VA00, u32x4{x130_0, x130_1, x131_0, x131_1}, Z4); SB();
      auto VA10 = VA10_r; auto VA11 = VA11_r; auto VA12 = VA12_r; asm volatile("" : "+v"(VA10), "+v"(VA11), "+v"(VA12)); SB();
      const f32x4 VAa0_k1 = mfma(VA01, u32x4{x132_0, x132_1, x133_0, x133_1}, VAa0_k0); SB();
      const f32x4 VAa0 = mfma(VA02, u32x4{x134_0, x134_1, x135_0, x135_1}, VAa0_k1); SB();
      auto VA20 = VA20_r; auto VA21 = VA21_r; auto VA22 = VA22_r; asm volatile("" : "+v"(VA20), "+v"(VA21), "+v"(VA22)); SB();
      const f32x4 VAa1_k0 = mfma(VA10, u32x4{x130_0, x130_1, x131_0, x131_1}, Z4); SB();
      const f32x4 VAa1_k1 = mfma(VA11, u32x4{x132_0, x132_1, x133_0, x133_1}, VAa1_k0); SB();
      const f32x4 VAa1 = mfma(VA12, u32x4{x134_0, x134_1, x135_0, x135_1}, VAa1_k1); SB();
      auto VA30 = VA30_r; auto VA31 = VA31_r; auto VA32 = VA32_r; asm volatile("" : "+v"(VA30), "+v"(VA31), "+v"(VA32)); SB();
      const f32x4 VAa2_k0 = mfma(VA20, u32x4{x130_0, x130_1, x131_0, x131_1}, Z4); SB();
      const f32x4 VAa2_k1 = mfma(VA21, u32x4{x132_0, x132_1, x133_0, x133_1}, VAa2_k0); SB();
      const f32x4 VAa2 = mfma(VA22, u32x4{x134_0, x134_1, x135_0, x135_1}, VAa2_k1); SB();
      auto VA40 = VA40_r; auto VA41 = VA41_r; auto VA42 = VA42_r; asm volatile("" : "+v"(VA40), "+v"(VA41), "+v"(VA42)); SB();
      const f32x4 VAa3_k0 = mfma(VA30, u32x4{x130_0, x130_1, x131_0, x131_1}, Z4); SB();
      const f32x4 VAa3_k1 = mfma(VA31, u32x4{x132_0, x132_1, x133_0, x133_1}, VAa3_k0); SB();
      const unsigned VAp0_0 = pk(VAa0[0], VAa0[1]); SB();
      const unsigned VAp0_1 = pk(VAa0[2], VAa0[3]); SB();
      const f32x4 VAa3 = mfma(VA32, u32x4{x134_0, x134_1, x135_0, x135_1}, VAa3_k1); SB();
      auto VA50 = VA50_r; auto VA51 = VA51_r; auto VA52 = VA52_r; asm volatile("" : "+v"(VA50), "+v"(VA51), "+v"(VA52)); SB();
      const unsigned VAp1_0 = pk(VAa1[0], VAa1[1]); SB();
      const f32x4 VAa4_k0 = mfma(VA40, u32x4{x130_0, x130_1, x131_0, x131_1}, Z4); SB();
      const unsigned VAp1_1 = pk(VAa1[2], VAa1[3]); SB();
      const unsigned VAp2_0 = pk(VAa2[0], VAa2[1]); SB();
      const f32x4 VAa4_k1 = mfma(VA41, u32x4{x132_0, x132_1, x133_0, x133_1}, VAa4_k0); SB();
      lds_st<u32x4>(spgA + 0, u32x4{VAp0_0, VAp0_1, VAp1_0, VAp1_1}); SB();
      const unsigned VAp2_1 = pk(VAa2[2], VAa2[3]); SB();
      const f32x4 VAa4 = mfma(VA42, u32x4{x134_0, x134_1, x135_0, x135_1}, VAa4_k1); SB();
      auto VB00 = VB00_r; auto VB01 = VB01_r; auto VB02 = VB02_r; asm volatile("" : "+v"(VB00), "+v"(VB01), "+v"(VB02)); SB();
      const unsigned VAp3_0 = pk(VAa3[0], VAa3[1]); SB();
      const f32x4 VAa5_k0 = mfma(VA50, u32x4{x130_0, x130_1, x131_0, x131_1}, Z4); SB();
      const u32x4 VB40_r = WFRAG2(1, 4, 0); SB();
      const unsigned VAp3_1 = pk(VAa3[2], VAa3[3]); SB();
      const f32x4 VAa5_k1 = mfma(VA51, u32x4{x132_0, x132_1, x133_0, x133_1}, VAa5_k0); SB();
      lds_st<u32x4>(spgA + 64, u32x4{VAp2_0, VAp2_1, VAp3_0, VAp3_1}); SB();
      const u32x4 VB41_r = WFRAG2(1, 4, 1); SB();
      const f32x4 VAa5 = mfma(VA52, u32x4{x134_0, x134_1, x135_0, x135_1}, VAa5_k1); SB();
      auto VB10 = VB10_r; auto VB11 = VB11_r; auto VB12 = VB12_r; asm volatile("" : "+v"(VB10), "+v"(VB11), "+v"(VB12)); SB();
      const unsigned VAp4_0 = pk(VAa4[0], VAa4[1]); SB();
      const f32x4 VBa0_k0 = mfma(VB00, u32x4{x130_0, x130_1, x131_0, x131_1}, Z4); SB();
      const u32x4 VB42_r = WFRAG2(1, 4, 2); SB();
      const unsigned VAp4_1 = pk(VAa4[2], VAa4[3]); SB();
      const f32x4 VBa0_k1 = mfma(VB01, u32x4{x132_0, x132_1, x133_0, x133_1}, VBa0_k0); SB();
      const u32x4 VB50_r = WFRAG2(1, 5, 0); SB();
      const u32x4 VB51_r = WFRAG2(1, 5, 1); SB();
      const f32x4 VBa0 = mfma(VB02, u32x4{x134_0, x134_1, x135_0, x135_1}, VBa0_k1); SB();
      auto VB20 = VB20_r; auto VB21 = VB21_r; auto VB22 = VB22_r; asm volatile("" : "+v"(VB20), "+v"(VB21), "+v"(VB22)); SB();
      const unsigned VAp5_0 = pk(VAa5[0], VAa5[1]); SB();
      const f32x4 VBa1_k0 = mfma(VB10, u32x4{x130_0, x130_1, x131_0, x131_1}, Z4); SB();
      const u32x4 VB52_r = WFRAG2(1, 5, 2); SB();
      const unsigned VAp5_1 = pk(VAa5[2], VAa5[3]); SB();
      const f32x4 VBa1_k1 = mfma(VB11, u32x4{x132_0, x132_1, x133_0, x133_1}, VBa1_k0); SB();
      lds_st<u32x4>(spgA + 128, u32x4{VAp4_0, VAp4_1, VAp5_0, VAp5_1}); SB();
      const f32x4 VBa1 = mfma(VB12, u32x4{x134_0, x134_1, x135_0, x135_1}, VBa1_k1); SB();
      auto VB30 = VB30_r; auto VB31 = VB31_r; auto VB32 = VB32_r; asm volatile("" : "+v"(VB30), "+v"(VB31), "+v"(VB32)); SB();
      const f32x4 VBa2_k0 = mfma(VB20, u32x4{x130_0, x130_1, x131_0, x131_1}, Z4); SB();
      const unsigned VBp0_0 = pk(VBa0[0], VBa0[1]); SB();
      const unsigned VBp0_1 = pk(VBa0[2], VBa0[3]); SB();
      const f32x4 VBa2_k1 = mfma(VB21, u32x4{x132_0, x132_1, x133_0, x133_1}, VBa2_k0); SB();
      const f32x4 VBa2 = mfma(VB22, u32x4{x134_0, x134_1, x135_0, x135_1}, VBa2_k1); SB();
      const f32x4 VBa3_k0 = mfma(VB30, u32x4{x130_0, x130_1, x131_0, x131_1}, Z4); SB();
      const f32x4 VBa3_k1 = mfma(VB31, u32x4{x132_0, x132_1, x133_0, x133_1}, VBa3_k0); SB();
      auto VB40 = VB40_r; auto VB41 = VB41_r; auto VB42 = VB42_r; asm volatile("" : "+v"(VB40), "+v"(VB41), "+v"(VB42)); SB();
      const f32x4 VBa3 = mfma(VB32, u32x4{x134_0, x134_1, x135_0, x135_1}, VBa3_k1); SB();
      const unsigned VBp1_0 = pk(VBa1[0], VBa1[1]); SB();
      const unsigned VBp1_1 = pk(VBa1[2], VBa1[3]); SB();
      const f32x4 VBa4_k0 = mfma(VB40, u32x4{x130_0, x130_1, x131_0, x131_1}, Z4); SB();
      lds_st<u32x4>(spuA + 0, u32x4{VBp0_0, VBp0_1, VBp1_0, VBp1_1}); SB();
      const f32x4 VBa4_k1 = mfma(VB41, u32x4{x132_0, x132_1, x133_0, x133_1}, VBa4_k0); SB();
      auto VB50 = VB50_r; auto VB51 = VB51_r; auto VB52 = VB52_r; asm volatile("" : "+v"(VB50), "+v"(VB51), "+v"(VB52)); SB();
      const unsigned VBp2_0 = pk(VBa2[0], VBa2[1]); SB();
      const f32x4 VBa4 = mfma(VB42, u32x4{x134_0, x134_1, x135_0, x135_1}, VBa4_k1); SB();
      const unsigned VBp2_1 = pk(VBa2[2], VBa2[3]); SB();
      const unsigned VBp3_0 = pk(VBa3[0], VBa3[1]); SB();
      const f32x4 VBa5_k0 = mfma(VB50, u32x4{x130_0, x130_1, x131_0, x131_1}, Z4); SB();
      const unsigned VBp3_1 = pk(VBa3[2], VBa3[3]); SB();
      const f32x4 VBa5_k1 = mfma(VB51, u32x4{x132_0, x132_1, x133_0, x133_1}, VBa5_k0); SB();
      lds_st<u32x4>(spuA + 64, u32x4{VBp2_0, VBp2_1, VBp3_0, VBp3_1}); SB();
      const f32x4 VBa5 = mfma(VB52, u32x4{x134_0, x134_1, x135_0, x135_1}, VBa5_k1); SB();
      const unsigned VBp4_0 = pk(VBa4[0], VBa4[1]); SB();
      const unsigned VBp4_1 = pk(VBa4[2], VBa4[3]); SB();
      const unsigned VBp5_0 = pk(VBa5[0], VBa5[1]); SB();
      const unsigned VBp5_1 = pk(VBa5[2], VBa5[3]); SB();
      lds_st<u32x4>(spuA + 128, u32x4{VBp4_0, VBp4_1, VBp5_0, VBp5_1}); SB();
      flag_arrive(lb + FLG + 16); SB();
      } else {
        SB();
      const u32x4 X00_r = lds_ld<u32x4>(RA(0) + 0); SB();
      const u32x4 X01_r = lds_ld<u32x4>(RA(0) + 64); SB();
      const u32x4 X02_r = lds_ld<u32x4>(RA(0) + 128); SB();
      const u32x4 WA00_r = WFRAG(0, 0, 0); SB();
      const u32x4 WA01_r = WFRAG(0, 0, 1); SB();
      const u32x4 WA02_r = WFRAG(0, 0, 2); SB();
      const u32x4 Z10_r = lds_ld<u32x4>(rb1 + 0); SB();
      const u32x4 Z11_r = lds_ld<u32x4>(rb1 + 64); SB();
      const u32x4 Z12_r = lds_ld<u32x4>(rb1 + 128); SB();
      const u32x4 Z20_r = lds_ld<u32x4>(rb2 + 0); SB();
      const u32x4 Z21_r = lds_ld<u32x4>(rb2 + 64); SB();
      const u32x4 Z22_r = lds_ld<u32x4>(rb2 + 128); SB();
      const u32x4 WA10_r = WFRAG(0, 1, 0); SB();
      const u32x4 WA11_r = WFRAG(0, 1, 1); SB();
      const u32x4 WA12_r = WFRAG(0, 1, 2); SB();
      const u32x4 X10_r = lds_ld<u32x4>(RA(1) + 0); SB();
      const u32x4 X11_r = lds_ld<u32x4>(RA(1) + 64); SB();
      const u32x4 X12_r = lds_ld<u32x4>(RA(1) + 128); SB();
      const u32x4 WA20_r = WFRAG(0, 2, 0); SB();
      const u32x4 WA21_r = WFRAG(0, 2, 1); SB();
      const u32x4 WA22_r = WFRAG(0, 2, 2); SB();
      const u32x4 WA30_r = WFRAG(0, 3, 0); SB();
      const u32x4 WA31_r = WFRAG(0, 3, 1); SB();
      const u32x4 WA32_r = WFRAG(0, 3, 2); SB();
      auto X00 = X00_r; auto X01 = X01_r; auto X02 = X02_r; asm volatile("" : "+v"(X00), "+v"(X01), "+v"(X02)); SB();
      const f32x4 G0_k0 = mfma(X00, X00, Z4); SB();
      auto WA00 = WA00_r; auto WA01 = WA01_r; auto WA02 = WA02_r; asm volatile("" : "+v"(WA00), "+v"(WA01), "+v"(WA02)); SB();
      const f32x4 ga00_k0 = mfma(WA00, X00, Z4); SB();
      auto Z10 = Z10_r; auto Z11 = Z11_r; auto Z12 = Z12_r; asm volatile("" : "+v"(Z10), "+v"(Z11), "+v"(Z12)); SB();
      const f32x4 ga01_k0 = mfma(WA00, Z10, Z4); SB();
      auto Z20 = Z20_r; auto Z21 = Z21_r; auto Z22 = Z22_r; asm volatile("" : "+v"(Z20), "+v"(Z21), "+v"(Z22)); SB();
      const f32x4 ga02_k0 = mfma(WA00, Z20, Z4); SB();
      const f32x4 G0_k1 = mfma(X01, X01, G0_k0); SB();
      const f32x4 ga00_k1 = mfma(WA01, X01, ga00_k0); SB();
      const f32x4 ga01_k1 = mfma(WA01, Z11, ga01_k0); SB();
      const f32x4 ga02_k1 = mfma(WA01, Z21, ga02_k0); SB();
      const f32x4 G0 = mfma(X02, X02, G0_k1); SB();
      const f32x4 ga00 = mfma(WA02, X02, ga00_k1); SB();
      auto WA10 = WA10_r; auto WA11 = WA11_r; auto WA12 = WA12_r; asm volatile("" : "+v"(WA10), "+v"(WA11), "+v"(WA12)); SB();
      const f32x4 ga01 = mfma(WA02, Z12, ga01_k1); SB();
      const f32x4 ga02 = mfma(WA02, Z22, ga02_k1); SB();
      const f32x4 Sm0_k0 = mfma(ONES, X00, Z4); SB();
      const f32x4 ga10_k0 = mfma(WA10, X00, Z4); SB();
      const f32x4 ga11_k0 = mfma(WA10, Z10, Z4); SB();
      const f32x4 ga12_k0 = mfma(WA10, Z20, Z4); SB();
      const f32x4 Sm0_k1 = mfma(ONES, X01, Sm0_k0); SB();
      const f32x4 ga10_k1 = mfma(WA11, X01, ga10_k0); SB();
      const unsigned gp00_0 = pk(ga00[0], ga00[1]); SB();
      const unsigned gp00_1 = pk(ga00[2], ga00[3]); SB();
      const f32x4 ga11_k1 = mfma(WA11, Z11, ga11_k0); SB();
      const unsigned gp10_0 = pk(ga01[0], ga01[1]); SB();
      const unsigned gp10_1 = pk(ga01[2], ga01[3]); SB();
      const f32x4 ga12_k1 = mfma(WA11, Z21, ga12_k0); SB();
      const unsigned gp20_0 = pk(ga02[0], ga02[1]); SB();
      const unsigned gp20_1 = pk(ga02[2], ga02[3]); SB();
      const f32x4 Sm0 = mfma(ONES, X02, Sm0_k1); SB();
      auto X10 = X10_r; auto X11 = X11_r; auto X12 = X12_r; asm volatile("" : "+v"(X10), "+v"(X11), "+v"(X12)); SB();
      const f32x4 ga10 = mfma(WA12, X02, ga10_k1); SB();
      auto WA20 = WA20_r; auto WA21 = WA21_r; auto WA22 = WA22_r; asm volatile("" : "+v"(WA20), "+v"(WA21), "+v"(WA22)); SB();
      const u32x4 X20_r = lds_ld<u32x4>(RA(2) + 0); SB();
      const f32x4 ga11 = mfma(WA12, Z12, ga11_k1); SB();
      const u32x4 X21_r = lds_ld<u32x4>(RA(2) + 64); SB();
      const u32x4 X22_r = lds_ld<u32x4>(RA(2) + 128); SB();
      const f32x4 ga12 = mfma(WA12, Z22, ga12_k1); SB();
      const u32x4 WA40_r = WFRAG(0, 4, 0); SB();
      const u32x4 WA41_r = WFRAG(0, 4, 1); SB();
      const f32x4 G1_k0 = mfma(X10, Z10, Z4); SB();
      const u32x4 WA42_r = WFRAG(0, 4, 2); SB();
      const unsigned gp01_0 = pk(ga10[0], ga10[1]); SB();
      const f32x4 ga20_k0 = mfma(WA20, X00, Z4); SB();
      const unsigned gp01_1 = pk(ga10[2], ga10[3]); SB();
      const unsigned gp11_0 = pk(ga11[0], ga11[1]); SB();
      const f32x4 ga21_k0 = mfma(WA20, Z10, Z4); SB();
      const unsigned gp11_1 = pk(ga11[2], ga11[3]); SB();
      const unsigned gp21_0 = pk(ga12[0], ga12[1]); SB();
      const f32x4 ga22_k0 = mfma(WA20, Z20, Z4); SB();
      const unsigned gp21_1 = pk(ga12[2], ga12[3]); SB();
      const f32x4 G1_k1 = mfma(X11, Z11, G1_k0); SB();
      const f32x4 ga20_k1 = mfma(WA21, X01, ga20_k0); SB();
      const f32x4 ga21_k1 = mfma(WA21, Z11, ga21_k0); SB();
      const f32x4 ga22_k1 = mfma(WA21, Z21, ga22_k0); SB();
      const f32x4 G1 = mfma(X12, Z12, G1_k1); SB();
      const f32x4 ga20 = mfma(WA22, X02, ga20_k1); SB();
      auto WA30 = WA30_r; auto WA31 = WA31_r; auto WA32 = WA32_r; asm volatile("" : "+v"(WA30), "+v"(WA31), "+v"(WA32)); SB();
      const f32x4 ga21 = mfma(WA22, Z12, ga21_k1); SB();
      const u32x4 WA50_r = WFRAG(0, 5, 0); SB();
      const u32x4 WA51_r = WFRAG(0, 5, 1); SB();
      const f32x4 ga22 = mfma(WA22, Z22, ga22_k1); SB();
      const u32x4 WA52_r = WFRAG(0, 5, 2); SB();
      const f32x4 Sm1_k0 = mfma(ONES, Z10, Z4); SB();
      const f32x4 ga30_k0 = mfma(WA30, X00, Z4); SB();
      const float Gm0_j1 = diag1 ? G1[0] : G0[0]; SB();
      const unsigned gp02_0 = pk(ga20[0], ga20[1]); SB();
      const f32x4 ga31_k0 = mfma(WA30, Z10, Z4); SB();
      const unsigned gp02_1 = pk(ga20[2], ga20[3]); SB();
      const unsigned gp12_0 = pk(ga21[0], ga21[1]); SB();
      const f32x4 ga32_k0 = mfma(WA30, Z20, Z4); SB();
      const unsigned gp12_1 = pk(ga21[2], ga21[3]); SB();
      const unsigned gp22_0 = pk(ga22[0], ga22[1]); SB();
      const f32x4 Sm1_k1 = mfma(ONES, Z11, Sm1_k0); SB();
      const unsigned gp22_1 = pk(ga22[2], ga22[3]); SB();
      const float Gm1_j1 = diag1 ? G1[1] : G0[1]; SB();
      const f32x4 ga30_k1 = mfma(WA31, X01, ga30_k0); SB();
      const float Gm2_j1 = diag1 ? G1[2] : G0[2]; SB();
      const float Gm3_j1 = diag1 ? G1[3] : G0[3]; SB();
      const f32x4 ga31_k1 = mfma(WA31, Z11, ga31_k0); SB();
      const f32x4 ga32_k1 = mfma(WA31, Z21, ga32_k0); SB();
      const f32x4 Sm1 = mfma(ONES, Z12, Sm1_k1); SB();
      auto X20 = X20_r; auto X21 = X21_r; auto X22 = X22_r; asm volatile("" : "+v"(X20), "+v"(X21), "+v"(X22)); SB();
      const f32x4 ga30 = mfma(WA32, X02, ga30_k1); SB();
      auto WA40 = WA40_r; auto WA41 = WA41_r; auto WA42 = WA42_r; asm volatile("" : "+v"(WA40), "+v"(WA41), "+v"(WA42)); SB();
      const u32x4 WB00_r = WFRAG(1, 0, 0); SB();
      const f32x4 ga31 = mfma(WA32, Z12, ga31_k1); SB();
      const u32x4 WB01_r = WFRAG(1, 0, 1); SB();
      const u32x4 WB02_r = WFRAG(1, 0, 2); SB();
      const f32x4 ga32 = mfma(WA32, Z22, ga32_k1); SB();
      const f32x4 G2_k0 = mfma(X20, Z20, Z4); SB();
      const f32x4 ga40_k0 = mfma(WA40, X00, Z4); SB();
      const float smm_j1 = diag1 ? Sm1[0] : Sm0[0]; SB();
      const unsigned gp03_0 = pk(ga30[0], ga30[1]); SB();
      const f32x4 ga41_k0 = mfma(WA40, Z10, Z4); SB();
      const unsigned gp03_1 = pk(ga30[2], ga30[3]); SB();
      const unsigned gp13_0 = pk(ga31[0], ga31[1]); SB();
      const f32x4 ga42_k0 = mfma(WA40, Z20, Z4); SB();
      const unsigned gp13_1 = pk(ga31[2], ga31[3]); SB();
      const unsigned gp23_0 = pk(ga32[0], ga32[1]); SB();
      const f32x4 G2_k1 = mfma(X21, Z21, G2_k0); SB();
      const unsigned gp23_1 = pk(ga32[2], ga32[3]); SB();
      const f32x4 ga40_k1 = mfma(WA41, X01, ga40_k0); SB();
      const f32x4 ga41_k1 = mfma(WA41, Z11, ga41_k0); SB();
      const f32x4 ga42_k1 = mfma(WA41, Z21, ga42_k0); SB();
      const f32x4 G2 = mfma(X22, Z22, G2_k1); SB();
      const f32x4 ga40 = mfma(WA42, X02, ga40_k1); SB();
      auto WA50 = WA50_r; auto WA51 = WA51_r; auto WA52 = WA52_r; asm volatile("" : "+v"(WA50), "+v"(WA51), "+v"(WA52)); SB();
      const f32x4 ga41 = mfma(WA42, Z12, ga41_k1); SB();
      const f32x4 ga42 = mfma(WA42, Z22, ga42_k1); SB();
      const f32x4 Sm2_k0 = mfma(ONES, Z20, Z4); SB();
      const f32x4 ga50_k0 = mfma(WA50, X00, Z4); SB();
      const f32x4 ga51_k0 = mfma(WA50, Z10, Z4); SB();
      const f32x4 ga52_k0 = mfma(WA50, Z20, Z4); SB();
      const u32x4 WB10_r = WFRAG(1, 1, 0); SB();
      const float Gm0 = diag2 ? G2[0] : Gm0_j1; SB();
      const f32x4 Sm2_k1 = mfma(ONES, Z21, Sm2_k0); SB();
      const u32x4 WB11_r = WFRAG(1, 1, 1); SB();
      const unsigned gp04_0 = pk(ga40[0], ga40[1]); SB();
      const f32x4 ga50_k1 = mfma(WA51, X01, ga50_k0); SB();
      const u32x4 WB12_r = WFRAG(1, 1, 2); SB();
      const unsigned gp04_1 = pk(ga40[2], ga40[3]); SB();
      const f32x4 ga51_k1 = mfma(WA51, Z11, ga51_k0); SB();
      const unsigned gp14_0 = pk(ga41[0], ga41[1]); SB();
      const unsigned gp14_1 = pk(ga41[2], ga41[3]); SB();
      const f32x4 ga52_k1 = mfma(WA51, Z21, ga52_k0); SB();
      const unsigned gp24_0 = pk(ga42[0], ga42[1]); SB();
      const unsigned gp24_1 = pk(ga42[2], ga42[3]); SB();
      const f32x4 Sm2 = mfma(ONES, Z22, Sm2_k1); SB();
      auto WB00 = WB00_r; auto WB01 = WB01_r; auto WB02 = WB02_r; asm volatile("" : "+v"(WB00), "+v"(WB01), "+v"(WB02)); SB();
      const float Gm1 = diag2 ? G2[1] : Gm1_j1; SB();
      const f32x4 ga50 = mfma(WA52, X02, ga50_k1); SB();
      const float Gm2 = diag2 ? G2[2] : Gm2_j1; SB();
      const float Gm3 = diag2 ? G2[3] : Gm3_j1; SB();
      const f32x4 ga51 = mfma(WA52, Z12, ga51_k1); SB();
      const float r_a = t_odd ? Gm1 : Gm0; SB();
      const float r_b = t_odd ? Gm3 : Gm2; SB();
      const f32x4 ga52 = mfma(WA52, Z22, ga52_k1); SB();
      const float smm = diag2 ? Sm2[0] : smm_j1; SB();
      KEEP(Sm0); KEEP(Sm1); KEEP(Sm2);
      const float r_q = t_hi ? r_b : r_a; SB();
      const f32x4 ua00_k0 = mfma(X00, WB00, Z4); SB();
      const u32x4 WB20_r = WFRAG(1, 2, 0); SB();
      const unsigned gp05_0 = pk(ga50[0], ga50[1]); SB();
      const f32x4 ua01_k0 = mfma(X10, WB00, Z4); SB();
      const u32x4 WB21_r = WFRAG(1, 2, 1); SB();
      const unsigned gp05_1 = pk(ga50[2], ga50[3]); SB();
      const f32x4 ua02_k0 = mfma(X20, WB00, Z4); SB();
      const u32x4 WB22_r = WFRAG(1, 2, 2); SB();
      const unsigned gp15_0 = pk(ga51[0], ga51[1]); SB();
      const f32x4 ua00_k1 = mfma(X01, WB01, ua00_k0); SB();
      const unsigned gp15_1 = pk(ga51[2], ga51[3]); SB();
      const unsigned gp25_0 = pk(ga52[0], ga52[1]); SB();
      const f32x4 ua01_k1 = mfma(X11, WB01, ua01_k0); SB();
      const unsigned gp25_1 = pk(ga52[2], ga52[3]); SB();
      const float r_s = smm * smm; SB();
      const f32x4 ua02_k1 = mfma(X21, WB01, ua02_k0); SB();
      const float r_d = fmaf(r_q, 96.f, -r_s); SB();
      const float r_w = max2(r_d, 0.f); SB();
      const f32x4 ua00 = mfma(X02, WB02, ua00_k1); SB();
      const float r_x = r_w + EPS96; SB();
      const float r_k = __builtin_amdgcn_fmed3f(r_w * 1e30f, 0.f, 1.f); SB();
      const f32x4 ua01 = mfma(X12, WB02, ua01_k1); SB();
      const float r_y = __builtin_amdgcn_rsqf(r_x); SB();
      const f32x4 ua02 = mfma(X22, WB02, ua02_k1); SB();
      const float r = fmaf(r_y, r_k, 1e-30f); SB();
      const f32x4 S0_k0 = mfma(X00, u32x4{gp00_0, gp00_1, gp01_0, gp01_1}, Z4); SB();
      const u32x4 WB30_r = WFRAG(1, 3, 0); SB();
      const unsigned up00_0 = pk(ua00[0], ua00[1]); SB();
      const f32x4 S1_k0 = mfma(X10, u32x4{gp10_0, gp10_1, gp11_0, gp11_1}, Z4); SB();
      const u32x4 WB31_r = WFRAG(1, 3, 1); SB();
      const unsigned up00_1 = pk(ua00[2], ua00[3]); SB();
      const f32x4 S2_k0 = mfma(X20, u32x4{gp20_0, gp20_1, gp21_0, gp21_1}, Z4); SB();
      const u32x4 WB32_r = WFRAG(1, 3, 2); SB();
      const unsigned up10_0 = pk(ua01[0], ua01[1]); SB();
      const f32x4 S0_k1 = mfma(X01, u32x4{gp02_0, gp02_1, gp03_0, gp03_1}, S0_k0); SB();
      const unsigned up10_1 = pk(ua01[2], ua01[3]); SB();
      const unsigned up20_0 = pk(ua02[0], ua02[1]); SB();
      const f32x4 S1_k1 = mfma(X11, u32x4{gp12_0, gp12_1, gp13_0, gp13_1}, S1_k0); SB();
      const unsigned up20_1 = pk(ua02[2], ua02[3]); SB();
      const f32x4 S2_k1 = mfma(X21, u32x4{gp22_0, gp22_1, gp23_0, gp23_1}, S2_k0); SB();
      auto WB10 = WB10_r; auto WB11 = WB11_r; auto WB12 = WB12_r; asm volatile("" : "+v"(WB10), "+v"(WB11), "+v"(WB12)); SB();
      const f32x4 S0 = mfma(X02, u32x4{gp04_0, gp04_1, gp05_0, gp05_1}, S0_k1); SB();
      const f32x4 S1 = mfma(X12, u32x4{gp14_0, gp14_1, gp15_0, gp15_1}, S1_k1); SB();
      const f32x4 S2 = mfma(X22, u32x4{gp24_0, gp24_1, gp25_0, gp25_1}, S2_k1); SB();
      const f32x4 ua10_k0 = mfma(X00, WB10, Z4); SB();
      const u32x4 WB40_r = WFRAG(1, 4, 0); SB();
      const u32x4 WB41_r = WFRAG(1, 4, 1); SB();
      const f32x4 ua11_k0 = mfma(X10, WB10, Z4); SB();
      const u32x4 WB42_r = WFRAG(1, 4, 2); SB();
      const f32x4 ua12_k0 = mfma(X20, WB10, Z4); SB();
      const f32x4 ua10_k1 = mfma(X01, WB11, ua10_k0); SB();
      const float Sx0_j1 = diag1 ? S1[0] : S0[0]; SB();
      const float Sx1_j1 = diag1 ? S1[1] : S0[1]; SB();
      const f32x4 ua11_k1 = mfma(X11, WB11, ua11_k0); SB();
      const float Sx2_j1 = diag1 ? S1[2] : S0[2]; SB();
      const float Sx0 = diag2 ? S2[0] : Sx0_j1; SB();
      const f32x4 ua12_k1 = mfma(X21, WB11, ua12_k0); SB();
      auto WB20 = WB20_r; auto WB21 = WB21_r; auto WB22 = WB22_r; asm volatile("" : "+v"(WB20), "+v"(WB21), "+v"(WB22)); SB();
      const float Sx1 = diag2 ? S2[1] : Sx1_j1; SB();
      const f32x4 ua10 = mfma(X02, WB12, ua10_k1); SB();
      const float Sx2 = diag2 ? S2[2] : Sx2_j1; SB();
      const float Sx3_j1 = diag1 ? S1[3] : S0[3]; SB();
      const f32x4 ua11 = mfma(X12, WB12, ua11_k1); SB();
      const float Sx3 = diag2 ? S2[3] : Sx3_j1; SB();
      const float Pm_t0 = Sx0 * dpp<0x00>(r); SB();
      const f32x4 ua12 = mfma(X22, WB12, ua12_k1); SB();
      const float Pm_t1 = Sx1 * dpp<0x55>(r); SB();
      const float Pm_t2 = Sx2 * dpp<0xAA>(r); SB();
      const f32x4 ua20_k0 = mfma(X00, WB20, Z4); SB();
      const u32x4 WB50_r = WFRAG(1, 5, 0); SB();
      const float Pm_t3 = Sx3 * dpp<0xFF>(r); SB();
      const f32x4 ua21_k0 = mfma(X10, WB20, Z4); SB();
      const u32x4 WB51_r = WFRAG(1, 5, 1); SB();
      const float Pm_m0 = max3(Pm_t0, Pm_t1, Pm_t2); SB();
      const f32x4 ua22_k0 = mfma(X20, WB20, Z4); SB();
      const u32x4 WB52_r = WFRAG(1, 5, 2); SB();
      const float Pm_m1 = max2(Pm_m0, Pm_t3); SB();
      const f32x4 ua20_k1 = mfma(X01, WB21, ua20_k0); SB();
      const float Pm_mn = -Pm_m1 * r; SB();
      const float Pm_z0 = fmaf(Pm_t0, r, Pm_mn); SB();
      const f32x4 ua21_k1 = mfma(X11, WB21, ua21_k0); SB();
      const float Pm_z1 = fmaf(Pm_t1, r, Pm_mn); SB();
      const float Pm_z2 = fmaf(Pm_t2, r, Pm_mn); SB();
      const f32x4 ua22_k1 = mfma(X21, WB21, ua22_k0); SB();
      auto WB30 = WB30_r; auto WB31 = WB31_r; auto WB32 = WB32_r; asm volatile("" : "+v"(WB30), "+v"(WB31), "+v"(WB32)); SB();
      const float Pm_z3 = fmaf(Pm_t3, r, Pm_mn); SB();
      const float Pm_r2 = opaque(r);
      const f32x4 ua20 = mfma(X02, WB22, ua20_k1); SB();
      const float Pm_e0 = exp2_(Pm_z0); SB();
      const f32x4 ua21 = mfma(X12, WB22, ua21_k1); SB();
      const float Pm_e1 = exp2_(Pm_z1); SB();
      const f32x4 ua22 = mfma(X22, WB22, ua22_k1); SB();
      const float Pm_e2 = exp2_(Pm_z2); SB();
      const f32x4 ua30_k0 = mfma(X00, WB30, Z4); SB();
      const u32x2 XT00_r = lds_tr(TA(0) + 0); SB();
      const float Pm_e3 = exp2_(Pm_z3); SB();
      const f32x4 ua31_k0 = mfma(X10, WB30, Z4); SB();
      const u32x2 XT10_r = lds_tr(TA(1) + 0); SB();
      const float Pm_l0 = Pm_e0 + Pm_e1; SB();
      const f32x4 ua32_k0 = mfma(X20, WB30, Z4); SB();
      const u32x2 XT20_r = lds_tr(TA(2) + 0); SB();
      const float Pm_l1 = Pm_e2 + Pm_e3; SB();
      const f32x4 ua30_k1 = mfma(X01, WB31, ua30_k0); SB();
      const u32x2 XT01_r = lds_tr(TA(0) + 8); SB();
      const float Pm_l = Pm_l0 + Pm_l1; SB();
      const f32x4 ua31_k1 = mfma(X11, WB31, ua31_k0); SB();
      const u32x2 XT11_r = lds_tr(TA(1) + 8); SB();
      const float Pm_i = __builtin_amdgcn_rcpf(Pm_l); SB();
      const f32x4 ua32_k1 = mfma(X21, WB31, ua32_k0); SB();
      auto WB40 = WB40_r; auto WB41 = WB41_r; auto WB42 = WB42_r; asm volatile("" : "+v"(WB40), "+v"(WB41), "+v"(WB42)); SB();
      const float Pm_im = Pm_i * 96.f; SB();
      const f32x4 ua30 = mfma(X02, WB32, ua30_k1); SB();
      const u32x2 XT21_r = lds_tr(TA(2) + 8); SB();
      const float Pm_w0 = dpp<0x00>(Pm_r2) * Pm_im; SB();
      const f32x4 ua31 = mfma(X12, WB32, ua31_k1); SB();
      const u32x2 XT02_r = lds_tr(TA(0) + 64); SB();
      const float Pm_w1 = dpp<0x55>(Pm_r2) * Pm_im; SB();
      const f32x4 ua32 = mfma(X22, WB32, ua32_k1); SB();
      const u32x2 XT12_r = lds_tr(TA(1) + 64); SB();
      const float Pm_w2 = dpp<0xAA>(Pm_r2) * Pm_im; SB();
      const f32x4 ua40_k0 = mfma(X00, WB40, Z4); SB();
      const u32x2 XT22_r = lds_tr(TA(2) + 64); SB();
      const float Pm_w3 = dpp<0xFF>(Pm_r2) * Pm_im; SB();
      const f32x4 ua41_k0 = mfma(X10, WB40, Z4); SB();
      const u32x2 XT03_r = lds_tr(TA(0) + 72); SB();
      const float Pm_p0 = Pm_e0 * Pm_w0; SB();
      const f32x4 ua42_k0 = mfma(X20, WB40, Z4); SB();
      const u32x2 XT13_r = lds_tr(TA(1) + 72); SB();
      const float Pm_p1 = Pm_e1 * Pm_w1; SB();
      const f32x4 ua40_k1 = mfma(X01, WB41, ua40_k0); SB();
      const u32x2 XT23_r = lds_tr(TA(2) + 72); SB();
      const float Pm_p2 = Pm_e2 * Pm_w2; SB();
      const f32x4 ua41_k1 = mfma(X11, WB41, ua41_k0); SB();
      const u32x2 XT04_r = lds_tr(TA(0) + 128); SB();
      const float Pm_p3 = Pm_e3 * Pm_w3; SB();
      const f32x4 ua42_k1 = mfma(X21, WB41, ua42_k0); SB();
      auto WB50 = WB50_r; auto WB51 = WB51_r; auto WB52 = WB52_r; asm volatile("" : "+v"(WB50), "+v"(WB51), "+v"(WB52)); SB();
      const unsigned Pm_u0 = pk(Pm_p0, Pm_p1); SB();
      const f32x4 ua40 = mfma(X02, WB42, ua40_k1); SB();
      const u32x2 XT05_r = lds_tr(TA(0) + 136); SB();
      const unsigned Pm_u1 = pk(Pm_p2, Pm_p3); SB();
      const f32x4 ua41 = mfma(X12, WB42, ua41_k1); SB();
      const u32x2 XT14_r = lds_tr(TA(1) + 128); SB();
      const unsigned P0_0 = diag0 ? Pm_u0 : 0u; SB();
      const f32x4 ua42 = mfma(X22, WB42, ua42_k1); SB();
      const u32x2 XT15_r = lds_tr(TA(1) + 136); SB();
      const unsigned id0_0 = diag0 ? IDR0 : 0u; SB();
      const f32x4 ua50_k0 = mfma(X00, WB50, Z4); SB();
      const u32x2 XT24_r = lds_tr(TA(2) + 128); SB();
      const unsigned P0_1 = diag0 ? Pm_u1 : 0u; SB();
      const f32x4 ua51_k0 = mfma(X10, WB50, Z4); SB();
      const u32x2 XT25_r = lds_tr(TA(2) + 136); SB();
      const unsigned id0_1 = diag0 ? IDR1 : 0u; SB();
      const f32x4 ua52_k0 = mfma(X20, WB50, Z4); SB();
      const unsigned P1_0 = diag1 ? Pm_u0 : 0u; SB();
      const unsigned id1_0 = diag1 ? IDR0 : 0u; SB();
      const f32x4 ua50_k1 = mfma(X01, WB51, ua50_k0); SB();
      const unsigned P1_1 = diag1 ? Pm_u1 : 0u; SB();
      const unsigned id1_1 = diag1 ? IDR1 : 0u; SB();
      const f32x4 ua51_k1 = mfma(X11, WB51, ua51_k0); SB();
      const unsigned P2_0 = diag2 ? Pm_u0 : 0u; SB();
      const unsigned id2_0 = diag2 ? IDR0 : 0u; SB();
      const f32x4 ua52_k1 = mfma(X21, WB51, ua52_k0); SB();
      auto XT00 = XT00_r; auto XT10 = XT10_r; auto XT20 = XT20_r; asm volatile("" : "+v"(XT00), "+v"(XT10), "+v"(XT20)); SB();
      const unsigned P2_1 = diag2 ? Pm_u1 : 0u; SB();
      const f32x4 ua50 = mfma(X02, WB52, ua50_k1); SB();
      const unsigned id2_1 = diag2 ? IDR1 : 0u; SB();
      const unsigned up01_0 = pk(ua10[0], ua10[1]); SB();
      const f32x4 ua51 = mfma(X12, WB52, ua51_k1); SB();
      const unsigned up01_1 = pk(ua10[2], ua10[3]); SB();
      const unsigned up11_0 = pk(ua11[0], ua11[1]); SB();
      const f32x4 ua52 = mfma(X22, WB52, ua52_k1); SB();
      auto XT01 = XT01_r; auto XT11 = XT11_r; auto XT21 = XT21_r; asm volatile("" : "+v"(XT01), "+v"(XT11), "+v"(XT21)); SB();
      const unsigned up11_1 = pk(ua11[2], ua11[3]); SB();
      const f32x4 o00 = mfma(u32x4{up00_0, up00_1, XT00[0], XT00[1]}, u32x4{P0_0, P0_1, id0_0, id0_1}, Z4); SB();
      const unsigned up21_0 = pk(ua12[0], ua12[1]); SB();
      const unsigned up21_1 = pk(ua12[2], ua12[3]); SB();
      const f32x4 o01 = mfma(u32x4{up10_0, up10_1, XT10[0], XT10[1]}, u32x4{P1_0, P1_1, id1_0, id1_1}, Z4); SB();
      const unsigned up02_0 = pk(ua20[0], ua20[1]); SB();
      const unsigned up02_1 = pk(ua20[2], ua20[3]); SB();
      const f32x4 o02 = mfma(u32x4{up20_0, up20_1, XT20[0], XT20[1]}, u32x4{P2_0, P2_1, id2_0, id2_1}, Z4); SB();
      auto XT02 = XT02_r; auto XT12 = XT12_r; auto XT22 = XT22_r; asm volatile("" : "+v"(XT02), "+v"(XT12), "+v"(XT22)); SB();
      const unsigned up12_0 = pk(ua21[0], ua21[1]); SB();
      const f32x4 o10 = mfma(u32x4{up01_0, up01_1, XT01[0], XT01[1]}, u32x4{P0_0, P0_1, id0_0, id0_1}, Z4); SB();
      const unsigned up12_1 = pk(ua21[2], ua21[3]); SB();
      const unsigned up22_0 = pk(ua22[0], ua22[1]); SB();
      const f32x4 o11 = mfma(u32x4{up11_0, up11_1, XT11[0], XT11[1]}, u32x4{P1_0, P1_1, id1_0, id1_1}, Z4); SB();
      const unsigned up22_1 = pk(ua22[2], ua22[3]); SB();
      const unsigned up03_0 = pk(ua30[0], ua30[1]); SB();
      const f32x4 o12 = mfma(u32x4{up21_0, up21_1, XT21[0], XT21[1]}, u32x4{P2_0, P2_1, id2_0, id2_1}, Z4); SB();
      auto XT03 = XT03_r; auto XT13 = XT13_r; auto XT23 = XT23_r; asm volatile("" : "+v"(XT03), "+v"(XT13), "+v"(XT23)); SB();
      const unsigned up03_1 = pk(ua30[2], ua30[3]); SB();
      const f32x4 o20 = mfma(u32x4{up02_0, up02_1, XT02[0], XT02[1]}, u32x4{P0_0, P0_1, id0_0, id0_1}, Z4); SB();
      const unsigned up13_0 = pk(ua31[0], ua31[1]); SB();
      const unsigned up13_1 = pk(ua31[2], ua31[3]); SB();
      const f32x4 o21 = mfma(u32x4{up12_0, up12_1, XT12[0], XT12[1]}, u32x4{P1_0, P1_1, id1_0, id1_1}, Z4); SB();
      const unsigned x100_0 = pk(o00[0], o00[1]); SB();
      const unsigned x100_1 = pk(o00[2], o00[3]); SB();
      const f32x4 o22 = mfma(u32x4{up22_0, up22_1, XT22[0], XT22[1]}, u32x4{P2_0, P2_1, id2_0, id2_1}, Z4); SB();
      const unsigned x101_0 = pk(o10[0], o10[1]); SB();
      const unsigned x101_1 = pk(o10[2], o10[3]); SB();
      const f32x4 o30 = mfma(u32x4{up03_0, up03_1, XT03[0], XT03[1]}, u32x4{P0_0, P0_1, id0_0, id0_1}, Z4); SB();
      lds_st<u32x4>(raE + 0, u32x4{x100_0, x100_1, x101_0, x101_1}); SB();
      const unsigned up23_0 = pk(ua32[0], ua32[1]); SB();
      const f32x4 o31 = mfma(u32x4{up13_0, up13_1, XT13[0], XT13[1]}, u32x4{P1_0, P1_1, id1_0, id1_1}, Z4); SB();
      const unsigned up23_1 = pk(ua32[2], ua32[3]); SB();
      const unsigned x110_0 = pk(o01[0], o01[1]); SB();
      const f32x4 G20_k0 = mfma(u32x4{x100_0, x100_1, x101_0, x101_1}, u32x4{x100_0, x100_1, x101_0, x101_1}, Z4); SB();
      const unsigned x110_1 = pk(o01[2], o01[3]); SB();
      const unsigned x111_0 = pk(o11[0], o11[1]); SB();
      const f32x4 o32 = mfma(u32x4{up23_0, up23_1, XT23[0], XT23[1]}, u32x4{P2_0, P2_1, id2_0, id2_1}, Z4); SB();
      auto XT04 = XT04_r; auto XT14 = XT14_r; auto XT24 = XT24_r; asm volatile("" : "+v"(XT04), "+v"(XT14), "+v"(XT24)); SB();
      const unsigned x111_1 = pk(o11[2], o11[3]); SB();
      const f32x4 S20_k0 = mfma(ONES, u32x4{x100_0, x100_1, x101_0, x101_1}, Z4); SB();
      lds_st<u32x4>(rb1 + 0, u32x4{x110_0, x110_1, x111_0, x111_1}); SB();
      const unsigned x120_0 = pk(o02[0], o02[1]); SB();
      const f32x4 G21_k0 = mfma(u32x4{x110_0, x110_1, x111_0, x111_1}, u32x4{x110_0, x110_1, x111_0, x111_1}, Z4); SB();
      const unsigned x120_1 = pk(o02[2], o02[3]); SB();
      const unsigned x121_0 = pk(o12[0], o12[1]); SB();
      const f32x4 S21_k0 = mfma(ONES, u32x4{x110_0, x110_1, x111_0, x111_1}, Z4); SB();
      const unsigned x121_1 = pk(o12[2], o12[3]); SB();
      const unsigned up04_0 = pk(ua40[0], ua40[1]); SB();
      const unsigned up04_1 = pk(ua40[2], ua40[3]); SB();
      const f32x4 G22_k0 = mfma(u32x4{x120_0, x120_1, x121_0, x121_1}, u32x4{x120_0, x120_1, x121_0, x121_1}, Z4); SB();
      lds_st<u32x4>(rb2 + 0, u32x4{x120_0, x120_1, x121_0, x121_1}); SB();
      const unsigned up14_0 = pk(ua41[0], ua41[1]); SB();
      const f32x4 o40 = mfma(u32x4{up04_0, up04_1, XT04[0], XT04[1]}, u32x4{P0_0, P0_1, id0_0, id0_1}, Z4); SB();
      const unsigned up14_1 = pk(ua41[2], ua41[3]); SB();
      const unsigned x102_0 = pk(o20[0], o20[1]); SB();
      const f32x4 S22_k0 = mfma(ONES, u32x4{x120_0, x120_1, x121_0, x121_1}, Z4); SB();
      const unsigned x102_1 = pk(o20[2], o20[3]); SB();
      const unsigned x103_0 = pk(o30[0], o30[1]); SB();
      const f32x4 o41 = mfma(u32x4{up14_0, up14_1, XT14[0], XT14[1]}, u32x4{P1_0, P1_1, id1_0, id1_1}, Z4); SB();
      const unsigned x103_1 = pk(o30[2], o30[3]); SB();
      const unsigned up24_0 = pk(ua42[0], ua42[1]); SB();
      const unsigned up24_1 = pk(ua42[2], ua42[3]); SB();
      const unsigned x112_0 = pk(o21[0], o21[1]); SB();
      const unsigned x112_1 = pk(o21[2], o21[3]); SB();
      const f32x4 o42 = mfma(u32x4{up24_0, up24_1, XT24[0], XT24[1]}, u32x4{P2_0, P2_1, id2_0, id2_1}, Z4); SB();
      lds_st<u32x4>(raE + 64, u32x4{x102_0, x102_1, x103_0, x103_1}); SB();
      const unsigned x113_0 = pk(o31[0], o31[1]); SB();
      const f32x4 G20_k1 = mfma(u32x4{x102_0, x102_1, x103_0, x103_1}, u32x4{x102_0, x102_1, x103_0, x103_1}, G20_k0); SB();
      auto XT05 = XT05_r; auto XT15 = XT15_r; auto XT25 = XT25_r; asm volatile("" : "+v"(XT05), "+v"(XT15), "+v"(XT25)); SB();
      const unsigned x113_1 = pk(o31[2], o31[3]); SB();
      const f32x4 S20_k1 = mfma(ONES, u32x4{x102_0, x102_1, x103_0, x103_1}, S20_k0); SB();
      lds_st<u32x4>(rb1 + 64, u32x4{x112_0, x112_1, x113_0, x113_1}); SB();
      const unsigned x122_0 = pk(o22[0], o22[1]); SB();
      const f32x4 G21_k1 = mfma(u32x4{x112_0, x112_1, x113_0, x113_1}, u32x4{x112_0, x112_1, x113_0, x113_1}, G21_k0); SB();
      const unsigned x122_1 = pk(o22[2], o22[3]); SB();
      const unsigned x123_0 = pk(o32[0], o32[1]); SB();
      const unsigned x123_1 = pk(o32[2], o32[3]); SB();
      const unsigned up05_0 = pk(ua50[0], ua50[1]); SB();
      const unsigned up05_1 = pk(ua50[2], ua50[3]); SB();
      const unsigned up15_0 = pk(ua51[0], ua51[1]); SB();
      const unsigned up15_1 = pk(ua51[2], ua51[3]); SB();
      const f32x4 o50 = mfma(u32x4{up05_0, up05_1, XT05[0], XT05[1]}, u32x4{P0_0, P0_1, id0_0, id0_1}, Z4); SB();
      lds_st<u32x4>(rb2 + 64, u32x4{x122_0, x122_1, x123_0, x123_1}); SB();
      const unsigned up25_0 = pk(ua52[0], ua52[1]); SB();
      const f32x4 o51 = mfma(u32x4{up15_0, up15_1, XT15[0], XT15[1]}, u32x4{P1_0, P1_1, id1_0, id1_1}, Z4); SB();
      const unsigned up25_1 = pk(ua52[2], ua52[3]); SB();
      const unsigned x104_0 = pk(o40[0], o40[1]); SB();
      const f32x4 S21_k1 = mfma(ONES, u32x4{x112_0, x112_1, x113_0, x113_1}, S21_k0); SB();
      const unsigned x104_1 = pk(o40[2], o40[3]); SB();
      const unsigned x114_0 = pk(o41[0], o41[1]); SB();
      const f32x4 o52 = mfma(u32x4{up25_0, up25_1, XT25[0], XT25[1]}, u32x4{P2_0, P2_1, id2_0, id2_1}, Z4); SB();
      const unsigned x105_0 = pk(o50[0], o50[1]); SB();
      const unsigned x105_1 = pk(o50[2], o50[3]); SB();
      const f32x4 G22_k1 = mfma(u32x4{x122_0, x122_1, x123_0, x123_1}, u32x4{x122_0, x122_1, x123_0, x123_1}, G22_k0); SB();
      lds_st<u32x4>(raE + 128, u32x4{x104_0, x104_1, x105_0, x105_1}); SB();
      const unsigned x114_1 = pk(o41[2], o41[3]); SB();
      const f32x4 S22_k1 = mfma(ONES, u32x4{x122_0, x122_1, x123_0, x123_1}, S22_k0); SB();
      const unsigned x115_0 = pk(o51[0], o51[1]); SB();
      const unsigned x115_1 = pk(o51[2], o51[3]); SB();
      const f32x4 G20 = mfma(u32x4{x104_0, x104_1, x105_0, x105_1}, u32x4{x104_0, x104_1, x105_0, x105_1}, G20_k1); SB();
      lds_st<u32x4>(rb1 + 128, u32x4{x114_0, x114_1, x115_0, x115_1}); SB();
      const unsigned x124_0 = pk(o42[0], o42[1]); SB();
      const f32x4 S20 = mfma(ONES, u32x4{x104_0, x104_1, x105_0, x105_1}, S20_k1); SB();
      const unsigned x124_1 = pk(o42[2], o42[3]); SB();
      const unsigned x125_0 = pk(o52[0], o52[1]); SB();
      const f32x4 G21 = mfma(u32x4{x114_0, x114_1, x115_0, x115_1}, u32x4{x114_0, x114_1, x115_0, x115_1}, G21_k1); SB();
      const unsigned x125_1 = pk(o52[2], o52[3]); SB();
      const f32x4 S21 = mfma(ONES, u32x4{x114_0, x114_1, x115_0, x115_1}, S21_k1); SB();
      lds_st<u32x4>(rb2 + 128, u32x4{x124_0, x124_1, x125_0, x125_1}); SB();
      const float q0_a = t_odd ? G20[1] : G20[0]; SB();
      const f32x4 G22 = mfma(u32x4{x124_0, x124_1, x125_0, x125_1}, u32x4{x124_0, x124_1, x125_0, x125_1}, G22_k1); SB();
      const float q0_b = t_odd ? G20[3] : G20[2]; SB();
      const float q0_q = t_hi ? q0_b : q0_a; SB();
      const f32x4 S22 = mfma(ONES, u32x4{x124_0, x124_1, x125_0, x125_1}, S22_k1); SB();
      const float q0_s = S20[0] * S20[0]; KEEP(S20); SB();
      const float q0_d = fmaf(q0_q, 96.f, -q0_s); SB();
      const float q0_w = max2(q0_d, 0.f); SB();
      const float q0_x = q0_w + EPS96; SB();
      const float q0_k = __builtin_amdgcn_fmed3f(q0_w * 1e30f, 0.f, 1.f); SB();
      const float q0_y = __builtin_amdgcn_rsqf(q0_x); SB();
      const float q1_a = t_odd ? G21[1] : G21[0]; SB();
      const float q0 = fmaf(q0_y, q0_k, 1e-30f); SB();
      const float qq0 = q0 * 96.f; SB();
      const float q1_b = t_odd ? G21[3] : G21[2]; SB();
      const float q1_q = t_hi ? q1_b : q1_a; SB();
      const float q1_s = S21[0] * S21[0]; KEEP(S21); SB();
      const float q1_d = fmaf(q1_q, 96.f, -q1_s); SB();
      const float q1_w = max2(q1_d, 0.f); SB();
      const float q1_x = q1_w + EPS96; SB();
      const float q1_k = __builtin_amdgcn_fmed3f(q1_w * 1e30f, 0.f, 1.f); SB();
      const float q1_y = __builtin_amdgcn_rsqf(q1_x); SB();
      const float q2_a = t_odd ? G22[1] : G22[0]; SB();
      const float q1 = fmaf(q1_y, q1_k, 1e-30f); SB();
      const float qq1 = q1 * 96.f; SB();
      const float q2_b = t_odd ? G22[3] : G22[2]; SB();
      const float q2_q = t_hi ? q2_b : q2_a; SB();
      const float q2_s = S22[0] * S22[0]; KEEP(S22); SB();
      const float q2_d = fmaf(q2_q, 96.f, -q2_s); SB();
      const float q2_w = max2(q2_d, 0.f); SB();
      const float q2_x = q2_w + EPS96; SB();
      const float q2_k = __builtin_amdgcn_fmed3f(q2_w * 1e30f, 0.f, 1.f); SB();
      const float q2_y = __builtin_amdgcn_rsqf(q2_x); SB();
      lds_st<float>(r2a0, qq0); SB();
      const float q2 = fmaf(q2_y, q2_k, 1e-30f); SB();
      const float qq2 = q2 * 96.f; SB();
      lds_st<float>(r2a1, qq1); SB();
      lds_st<float>(r2a2, qq2); SB();
      flag_arrive(fB1); SB();
      }
    }
    flag_wait2(lb + FLG + 16, 1, fB1, 4 * (n + 1));
    PRIO_S2();
    {
      constexpr int WMASK = 1;
      const int tok = 4 * wl4 + w;
      const unsigned wl = opaque(lb + W_OFF + 36864 + lane * 16);
      const unsigned r2f = r2f0 + n * 1024;
      const unsigned r2r = r2f + g * 16, r2c = r2f + i * 4;
      const unsigned rsa = lb + SPR + tok * 4;
      const unsigned rda = g < 3 ? r2f + lane * 4 : rsa;
      const unsigned spa = lb + SPX + tok * TOKB + 16 * g;
      const unsigned sua = lb + SPU + tok * TOKB + 2 * chan(0, i);
      const unsigned osoff = obase + wl4 * 7 * TOKB;
      const __amdgpu_buffer_rsrc_t ors = __builtin_amdgcn_make_buffer_rsrc(out + osoff, 0, 4 * FRAMEB - osoff, 0x00020000);
      SB();
      const u32x4 Y00_r = lds_ld<u32x4>(rb2 + 0); SB();
      const u32x4 Y01_r = lds_ld<u32x4>(rb2 + 64); SB();
      const u32x4 Y02_r = lds_ld<u32x4>(rb2 + 128); SB();
      const u32x4 WA00_r = WFRAG(0, 0, 0); SB();
      const u32x4 WA01_r = WFRAG(0, 0, 1); SB();
      const u32x4 WA02_r = WFRAG(0, 0, 2); SB();
      const u32x4 Y10_r = lds_ld<u32x4>(rb2 + 3072); SB();
      const u32x4 Y11_r = lds_ld<u32x4>(rb2 + 3136); SB();
      const u32x4 Y12_r = lds_ld<u32x4>(rb2 + 3200); SB();
      const u32x4 Y20_r = lds_ld<u32x4>(rb2 + 6144); SB();
      const u32x4 Y21_r = lds_ld<u32x4>(rb2 + 6208); SB();
      const u32x4 Y22_r = lds_ld<u32x4>(rb2 + 6272); SB();
      const float rd = lds_ld<float>(rda); SB();
      const float rs = lds_ld<float>(rsa); SB();
      const u32x4 GS0_r = lds_ld<u32x4>(spa + 3072); SB();
      const u32x4 GS1_r = lds_ld<u32x4>(spa + 3136); SB();
      const u32x4 GS2_r = lds_ld<u32x4>(spa + 3200); SB();
      const u32x4 WA10_r = WFRAG(0, 1, 0); SB();
      const u32x4 WA11_r = WFRAG(0, 1, 1); SB();
      const u32x4 WA12_r = WFRAG(0, 1, 2); SB();
      const u32x4 XS0_r = lds_ld<u32x4>(spa + 0); SB();
      const u32x4 XS1_r = lds_ld<u32x4>(spa + 64); SB();
      const u32x4 XS2_r = lds_ld<u32x4>(spa + 128); SB();
      const u32x4 WA20_r = WFRAG(0, 2, 0); SB();
      const u32x4 WA21_r = WFRAG(0, 2, 1); SB();
      const u32x4 WA22_r = WFRAG(0, 2, 2); SB();
      auto Y00 = Y00_r; auto Y01 = Y01_r; auto Y02 = Y02_r; asm volatile("" : "+v"(Y00), "+v"(Y01), "+v"(Y02)); SB();
      auto WA00 = WA00_r; auto WA01 = WA01_r; auto WA02 = WA02_r; asm volatile("" : "+v"(WA00), "+v"(WA01), "+v"(WA02)); SB();
      const f32x4 ga00_k0 = mfma(WA00, Y00, Z4); SB();
      auto Y10 = Y10_r; auto Y11 = Y11_r; auto Y12 = Y12_r; asm volatile("" : "+v"(Y10), "+v"(Y11), "+v"(Y12)); SB();
      flag_arrive(fB2); SB();
      const f32x4 ga01_k0 = mfma(WA00, Y10, Z4); SB();
      auto Y20 = Y20_r; auto Y21 = Y21_r; auto Y22 = Y22_r; asm volatile("" : "+v"(Y20), "+v"(Y21), "+v"(Y22)); SB();
      const unsigned b2seen = *(volatile AS3 unsigned*)fB2; SB();
      const f32x4 ga02_k0 = mfma(WA00, Y20, Z4); SB();
      const f32x4 ga00_k1 = mfma(WA01, Y01, ga00_k0); SB();
      const f32x4 ga01_k1 = mfma(WA01, Y11, ga01_k0); SB();
      const f32x4 ga02_k1 = mfma(WA01, Y21, ga02_k0); SB();
      auto GS0 = GS0_r; auto GS1 = GS1_r; auto GS2 = GS2_r; asm volatile("" : "+v"(GS0), "+v"(GS1), "+v"(GS2)); SB();
      const f32x4 ga00 = mfma(WA02, Y02, ga00_k1); SB();
      const f32x4 ga01 = mfma(WA02, Y12, ga01_k1); SB();
      const f32x4 ga02 = mfma(WA02, Y22, ga02_k1); SB();
      const f32x4 Sd0_k0 = mfma(GS0, Y00, Z4); SB();
      const f32x4 Sd0_k1 = mfma(GS1, Y01, Sd0_k0); SB();
      const f32x4 Sd0 = mfma(GS2, Y02, Sd0_k1); SB();
      auto WA10 = WA10_r; auto WA11 = WA11_r; auto WA12 = WA12_r; asm volatile("" : "+v"(WA10), "+v"(WA11), "+v"(WA12)); SB();
      const f32x4 Sd1_k0 = mfma(GS0, Y10, Z4); SB();
      const u32x4 WA30_r = WFRAG(0, 3, 0); SB();
      const u32x4 WA31_r = WFRAG(0, 3, 1); SB();
      const f32x4 Sd1_k1 = mfma(GS1, Y11, Sd1_k0); SB();
      const u32x4 WA32_r = WFRAG(0, 3, 2); SB();
      const unsigned gp00_0 = pk(ga00[0], ga00[1]); SB();
      const f32x4 Sd1 = mfma(GS2, Y12, Sd1_k1); SB();
      const unsigned gp00_1 = pk(ga00[2], ga00[3]); SB();
      const unsigned gp10_0 = pk(ga01[0], ga01[1]); SB();
      const f32x4 ga10_k0 = mfma(WA10, Y00, Z4); SB();
      const unsigned gp10_1 = pk(ga01[2], ga01[3]); SB();
      const unsigned gp20_0 = pk(ga02[0], ga02[1]); SB();
      const f32x4 ga11_k0 = mfma(WA10, Y10, Z4); SB();
      const unsigned gp20_1 = pk(ga02[2], ga02[3]); SB();
      const f32x4 ga12_k0 = mfma(WA10, Y20, Z4); SB();
      const float d_a = g_odd ? Sd1[0] : Sd0[0]; KEEP(Sd0); KEEP(Sd1); SB();
      const f32x4 ga10_k1 = mfma(WA11, Y01, ga10_k0); SB();
      const f32x4 ga11_k1 = mfma(WA11, Y11, ga11_k0); SB();
      const f32x4 ga12_k1 = mfma(WA11, Y21, ga12_k0); SB();
      const f32x4 ga10 = mfma(WA12, Y02, ga10_k1); SB();
      const u32x4 WA40_r = WFRAG(0, 4, 0); SB();
      const u32x4 WA41_r = WFRAG(0, 4, 1); SB();
      const f32x4 ga11 = mfma(WA12, Y12, ga11_k1); SB();
      flag_wait_seen(b2seen, fB2, 4, (unsigned)n); DMA_TILE(0); SB();
      const f32x4 ga12 = mfma(WA12, Y22, ga12_k1); SB();
      auto XS0 = XS0_r; auto XS1 = XS1_r; auto XS2 = XS2_r; asm volatile("" : "+v"(XS0), "+v"(XS1), "+v"(XS2)); SB();
      const u32x4 WA42_r = WFRAG(0, 4, 2); SB();
      const f32x4 Sd2_k0 = mfma(GS0, Y20, Z4); SB();
      const f32x4 Sd2_k1 = mfma(GS1, Y21, Sd2_k0); SB();
      const f32x4 Sd2 = mfma(GS2, Y22, Sd2_k1); SB();
      auto WA20 = WA20_r; auto WA21 = WA21_r; auto WA22 = WA22_r; asm volatile("" : "+v"(WA20), "+v"(WA21), "+v"(WA22)); SB();
      const unsigned gp01_0 = pk(ga10[0], ga10[1]); SB();
      const f32x4 Sd3_k0 = mfma(GS0, XS0, Z4); SB();
      const unsigned gp01_1 = pk(ga10[2], ga10[3]); SB();
      const unsigned gp11_0 = pk(ga11[0], ga11[1]); SB();
      const f32x4 Sd3_k1 = mfma(GS1, XS1, Sd3_k0); SB();
      const unsigned gp11_1 = pk(ga11[2], ga11[3]); SB();
      const unsigned gp21_0 = pk(ga12[0], ga12[1]); SB();
      const f32x4 Sd3 = mfma(GS2, XS2, Sd3_k1); SB();
      const unsigned gp21_1 = pk(ga12[2], ga12[3]); SB();
      const f32x4 ga20_k0 = mfma(WA20, Y00, Z4); SB();
      const u32x4 WA50_r = WFRAG(0, 5, 0); SB();
      const u32x4 WA51_r = WFRAG(0, 5, 1); SB();
      const f32x4 ga21_k0 = mfma(WA20, Y10, Z4); SB();
      const u32x4 WA52_r = WFRAG(0, 5, 2); SB();
      const f32x4 ga22_k0 = mfma(WA20, Y20, Z4); SB();
      const f32x4 ga20_k1 = mfma(WA21, Y01, ga20_k0); SB();
      const float d_b = g_odd ? Sd3[0] : Sd2[0]; KEEP(Sd2); KEEP(Sd3); SB();
      const float d_c = g_hi ? d_b : d_a; SB();
      const f32x4 ga21_k1 = mfma(WA21, Y11, ga21_k0); SB();
      DMA_TILE(1); SB();
      const f32x4 ga22_k1 = mfma(WA21, Y21, ga22_k0); SB();
      auto WA30 = WA30_r; auto WA31 = WA31_r; auto WA32 = WA32_r; asm volatile("" : "+v"(WA30), "+v"(WA31), "+v"(WA32)); SB();
      const float d_t = fmaf(d_c, rd, biasd); SB();
      const f32x4 ga20 = mfma(WA22, Y02, ga20_k1); SB();
      const f32x4 ga21 = mfma(WA22, Y12, ga21_k1); SB();
      const float d_m0_i0 = max2(d_t, dpp<0x128>(d_t)); SB();
      const f32x4 ga22 = mfma(WA22, Y22, ga22_k1); SB();
      const f32x4 ga30_k0 = mfma(WA30, Y00, Z4); SB();
      const float rc0 = lds_ld<float>(r2c + 0); SB();
      const float d_m0_i1 = max2(d_m0_i0, dpp<0x124>(d_m0_i0)); SB();
      const f32x4 ga31_k0 = mfma(WA30, Y10, Z4); SB();
      const f32x4 Rr0_r = lds_ld<f32x4>(r2r + 0); SB();
      const unsigned gp02_0 = pk(ga20[0], ga20[1]); SB();
      const f32x4 ga32_k0 = mfma(WA30, Y20, Z4); SB();
      const f32x4 Rr1_r = lds_ld<f32x4>(r2r + 64); SB();
      const unsigned gp02_1 = pk(ga20[2], ga20[3]); SB();
      const f32x4 ga30_k1 = mfma(WA31, Y01, ga30_k0); SB();
      const f32x4 Rr2_r = lds_ld<f32x4>(r2r + 128); SB();
      const unsigned gp12_0 = pk(ga21[0], ga21[1]); SB();
      const f32x4 ga31_k1 = mfma(WA31, Y11, ga31_k0); SB();
      const unsigned gp12_1 = pk(ga21[2], ga21[3]); SB();
      const unsigned gp22_0 = pk(ga22[0], ga22[1]); SB();
      const f32x4 ga32_k1 = mfma(WA31, Y21, ga32_k0); SB();
      auto WA40 = WA40_r; auto WA41 = WA41_r; auto WA42 = WA42_r; asm volatile("" : "+v"(WA40), "+v"(WA41), "+v"(WA42)); SB();
      const unsigned gp22_1 = pk(ga22[2], ga22[3]); SB();
      const f32x4 ga30 = mfma(WA32, Y02, ga30_k1); SB();
      const float d_m0_i2 = max2(d_m0_i1, dpp<0x122>(d_m0_i1)); SB();
      const f32x4 ga31 = mfma(WA32, Y12, ga31_k1); SB();
      DMA_TILE(2); SB();
      const f32x4 ga32 = mfma(WA32, Y22, ga32_k1); SB();
      const float d_m0 = max2(d_m0_i2, dpp<0x121>(d_m0_i2)); SB();
      const f32x4 ga40_k0 = mfma(WA40, Y00, Z4); SB();
      const unsigned d_m_x0_c = vcopy(f2u(d_m0)); SB();
      const f32x4 ga41_k0 = mfma(WA40, Y10, Z4); SB();
      const f32x4 ga42_k0 = mfma(WA40, Y20, Z4); SB();
      const unsigned gp03_0 = pk(ga30[0], ga30[1]); SB();
      const unsigned gp03_1 = pk(ga30[2], ga30[3]); SB();
      const f32x4 ga40_k1 = mfma(WA41, Y01, ga40_k0); SB();
      const float rc1 = lds_ld<float>(r2c + 64); SB();
      const unsigned gp13_0 = pk(ga31[0], ga31[1]); SB();
      const f32x4 ga41_k1 = mfma(WA41, Y11, ga41_k0); SB();
      const unsigned gp13_1 = pk(ga31[2], ga31[3]); SB();
      const unsigned gp23_0 = pk(ga32[0], ga32[1]); SB();
      const f32x4 ga42_k1 = mfma(WA41, Y21, ga42_k0); SB();
      auto WA50 = WA50_r; auto WA51 = WA51_r; auto WA52 = WA52_r; asm volatile("" : "+v"(WA50), "+v"(WA51), "+v"(WA52)); SB();
      const unsigned gp23_1 = pk(ga32[2], ga32[3]); SB();
      const f32x4 ga40 = mfma(WA42, Y02, ga40_k1); SB();
      const auto d_m_x0 = __builtin_amdgcn_permlane16_swap(f2u(d_m0), d_m_x0_c, false, false); const unsigned d_m_x0_a = d_m_x0[0], d_m_x0_b = d_m_x0[1]; SB();
      const f32x4 ga41 = mfma(WA42, Y12, ga41_k1); SB();
      const float d_m_r = max2(u2f(d_m_x0_a), u2f(d_m_x0_b)); SB();
      const unsigned d_m_x1_c = vcopy(f2u(d_m_r)); SB();
      const f32x4 ga42 = mfma(WA42, Y22, ga42_k1); SB();
      const f32x4 ga50_k0 = mfma(WA50, Y00, Z4); SB();
      const auto d_m_x1 = __builtin_amdgcn_permlane32_swap(f2u(d_m_r), d_m_x1_c, false, false); const unsigned d_m_x1_a = d_m_x1[0], d_m_x1_b = d_m_x1[1]; SB();
      const f32x4 ga51_k0 = mfma(WA50, Y10, Z4); SB();
      const unsigned gp04_0 = pk(ga40[0], ga40[1]); SB();
      const unsigned gp04_1 = pk(ga40[2], ga40[3]); SB();
      const f32x4 ga52_k0 = mfma(WA50, Y20, Z4); SB();
      const unsigned gp14_0 = pk(ga41[0], ga41[1]); SB();
      const unsigned gp14_1 = pk(ga41[2], ga41[3]); SB();
      const f32x4 ga50_k1 = mfma(WA51, Y01, ga50_k0); SB();
      const unsigned gp24_0 = pk(ga42[0], ga42[1]); SB();
      const unsigned gp24_1 = pk(ga42[2], ga42[3]); SB();
      const f32x4 ga51_k1 = mfma(WA51, Y11, ga51_k0); SB();
      const float d_m = max2(u2f(d_m_x1_a), u2f(d_m_x1_b)); SB();
      const float d_mn = -d_m * rs; SB();
      const f32x4 ga52_k1 = mfma(WA51, Y21, ga52_k0); SB();
      const float d_z = fmaf(d_t, rs, d_mn); SB();
      const float d_e = exp2_(d_z); SB();
      const f32x4 ga50 = mfma(WA52, Y02, ga50_k1); SB();
      const u32x4 WB00_r = WFRAG(1, 0, 0); SB();
      const float d_l0_i0 = d_e + dpp<0x128>(d_e); SB();
      const f32x4 ga51 = mfma(WA52, Y12, ga51_k1); SB();
      const u32x4 WB01_r = WFRAG(1, 0, 1); SB();
      const float d_l0_i1 = d_l0_i0 + dpp<0x124>(d_l0_i0); SB();
      const f32x4 ga52 = mfma(WA52, Y22, ga52_k1); SB();
      const u32x4 WB02_r = WFRAG(1, 0, 2); SB();
      const float d_l0_i2 = d_l0_i1 + dpp<0x122>(d_l0_i1); SB();
      const f32x4 S00_k0 = mfma(Y00, u32x4{gp00_0, gp00_1, gp01_0, gp01_1}, Z4); SB();
      PRIO_SET(0); SB();
      const unsigned gp05_0 = pk(ga50[0], ga50[1]); SB();
      const f32x4 S01_k0 = mfma(Y10, u32x4{gp00_0, gp00_1, gp01_0, gp01_1}, Z4); SB();
      const unsigned gp05_1 = pk(ga50[2], ga50[3]); SB();
      const unsigned gp15_0 = pk(ga51[0], ga51[1]); SB();
      const f32x4 S02_k0 = mfma(Y20, u32x4{gp00_0, gp00_1, gp01_0, gp01_1}, Z4); SB();
      auto Rr0 = Rr0_r; auto Rr1 = Rr1_r; auto Rr2 = Rr2_r; asm volatile("" : "+v"(Rr0), "+v"(Rr1), "+v"(Rr2)); SB();
      const unsigned gp15_1 = pk(ga51[2], ga51[3]); SB();
      const f32x4 S03_k0 = mfma(XS0, u32x4{gp00_0, gp00_1, gp01_0, gp01_1}, Z4); SB();
      const unsigned gp25_0 = pk(ga52[0], ga52[1]); SB();
      const unsigned gp25_1 = pk(ga52[2], ga52[3]); SB();
      const f32x4 S00_k1 = mfma(Y01, u32x4{gp02_0, gp02_1, gp03_0, gp03_1}, S00_k0); SB();
      const float d_l0 = d_l0_i2 + dpp<0x121>(d_l0_i2); SB();
      const unsigned d_l_x0_c = vcopy(f2u(d_l0)); SB();
      const f32x4 S01_k1 = mfma(Y11, u32x4{gp02_0, gp02_1, gp03_0, gp03_1}, S01_k0); SB();
      const float d_p = d_e * rd; SB();
      const auto d_l_x0 = __builtin_amdgcn_permlane16_swap(f2u(d_l0), d_l_x0_c, false, false); const unsigned d_l_x0_a = d_l_x0[0], d_l_x0_b = d_l_x0[1]; SB();
      const f32x4 S02_k1 = mfma(Y21, u32x4{gp02_0, gp02_1, gp03_0, gp03_1}, S02_k0); SB();
      const u32x4 WB10_r = WFRAG(1, 1, 0); SB();
      const float d_l_r = u2f(d_l_x0_a) + u2f(d_l_x0_b); SB();
      const f32x4 S03_k1 = mfma(XS1, u32x4{gp02_0, gp02_1, gp03_0, gp03_1}, S03_k0); SB();
      const u32x4 WB11_r = WFRAG(1, 1, 1); SB();
      const unsigned d_l_x1_c = vcopy(f2u(d_l_r)); SB();
      const f32x4 S00 = mfma(Y02, u32x4{gp04_0, gp04_1, gp05_0, gp05_1}, S00_k1); SB();
      const u32x4 WB12_r = WFRAG(1, 1, 2); SB();
      const auto d_l_x1 = __builtin_amdgcn_permlane32_swap(f2u(d_l_r), d_l_x1_c, false, false); const unsigned d_l_x1_a = d_l_x1[0], d_l_x1_b = d_l_x1[1]; SB();
      const f32x4 S01 = mfma(Y12, u32x4{gp04_0, gp04_1, gp05_0, gp05_1}, S01_k1); SB();
      const float d_l = u2f(d_l_x1_a) + u2f(d_l_x1_b); SB();
      const float d_i = __builtin_amdgcn_rcpf(d_l); SB();
      const f32x4 S02 = mfma(Y22, u32x4{gp04_0, gp04_1, gp05_0, gp05_1}, S02_k1); SB();
      const float d_q = d_p * d_i; SB();
      const unsigned d_k = pk(d_q, 0.f); SB();
      const f32x4 S03 = mfma(XS2, u32x4{gp04_0, gp04_1, gp05_0, gp05_1}, S03_k1); SB();
      lds_st<unsigned short>(pscw, (unsigned short)d_k); SB();
      const float P0_t00 = S00[0] * Rr0[0]; SB();
      const f32x4 S10_k0 = mfma(Y00, u32x4{gp10_0, gp10_1, gp11_0, gp11_1}, Z4); SB();
      const float P0_t01 = S00[1] * Rr0[1]; SB();
      const float P0_t02 = S00[2] * Rr0[2]; SB();
      const f32x4 S11_k0 = mfma(Y10, u32x4{gp10_0, gp10_1, gp11_0, gp11_1}, Z4); SB();
      const float P0_t03 = S00[3] * Rr0[3]; SB();
      const float P0_t10 = S01[0] * Rr1[0]; SB();
      const f32x4 S12_k0 = mfma(Y20, u32x4{gp10_0, gp10_1, gp11_0, gp11_1}, Z4); SB();
      const float P0_t11 = S01[1] * Rr1[1]; SB();
      const float P0_t12 = S01[2] * Rr1[2]; SB();
      const f32x4 S13_k0 = mfma(XS0, u32x4{gp10_0, gp10_1, gp11_0, gp11_1}, Z4); SB();
      const u32x4 WB20_r = WFRAG(1, 2, 0); SB();
      const float P0_t13 = S01[3] * Rr1[3]; SB();
      const f32x4 S10_k1 = mfma(Y01, u32x4{gp12_0, gp12_1, gp13_0, gp13_1}, S10_k0); SB();
      const u32x4 WB21_r = WFRAG(1, 2, 1); SB();
      const float P0_t20 = S02[0] * Rr2[0]; SB();
      const f32x4 S11_k1 = mfma(Y11, u32x4{gp12_0, gp12_1, gp13_0, gp13_1}, S11_k0); SB();
      const u32x4 WB22_r = WFRAG(1, 2, 2); SB();
      const float P0_t21 = S02[1] * Rr2[1]; SB();
      const f32x4 S12_k1 = mfma(Y21, u32x4{gp12_0, gp12_1, gp13_0, gp13_1}, S12_k0); SB();
      const float P0_t22 = S02[2] * Rr2[2]; SB();
      const float P0_t23 = S02[3] * Rr2[3]; SB();
      const f32x4 S13_k1 = mfma(XS1, u32x4{gp12_0, gp12_1, gp13_0, gp13_1}, S13_k0); SB();
      const float P0_m0 = max3(P0_t00, P0_t01, P0_t02); SB();
      const float P0_m1 = max3(P0_t03, P0_t10, P0_t11); SB();
      const f32x4 S10 = mfma(Y02, u32x4{gp14_0, gp14_1, gp15_0, gp15_1}, S10_k1); SB();
      auto WB00 = WB00_r; auto WB01 = WB01_r; auto WB02 = WB02_r; asm volatile("" : "+v"(WB00), "+v"(WB01), "+v"(WB02)); SB();
      const float P0_m2 = max3(P0_t12, P0_t13, P0_t20); SB();
      const f32x4 S11 = mfma(Y12, u32x4{gp14_0, gp14_1, gp15_0, gp15_1}, S11_k1); SB();
      const float P0_tx = fmaf(S03[0], rs, biask); KEEP(S03); SB();
      const float P0_m3 = max3(P0_t21, P0_t22, P0_t23); SB();
      const f32x4 S12 = mfma(Y22, u32x4{gp14_0, gp14_1, gp15_0, gp15_1}, S12_k1); SB();
      const float P0_m4 = max3(P0_m0, P0_m1, P0_m2); SB();
      const float P0_m5 = max3(P0_m4, P0_m3, P0_tx); SB();
      const f32x4 S13 = mfma(XS2, u32x4{gp14_0, gp14_1, gp15_0, gp15_1}, S13_k1); SB();
      const unsigned P0_mx_x0_c = vcopy(f2u(P0_m5)); SB();
      const float P1_t00 = S10[0] * Rr0[0]; SB();
      const f32x4 ua00_k0 = mfma(Y00, WB00, Z4); SB();
      const u32x4 WB30_r = WFRAG(1, 3, 0); SB();
      const auto P0_mx_x0 = __builtin_amdgcn_permlane16_swap(f2u(P0_m5), P0_mx_x0_c, false, false); const unsigned P0_mx_x0_a = P0_mx_x0[0], P0_mx_x0_b = P0_mx_x0[1]; SB();
      const f32x4 ua01_k0 = mfma(Y10, WB00, Z4); SB();
      const u32x4 WB31_r = WFRAG(1, 3, 1); SB();
      const float P0_mx_r = max2(u2f(P0_mx_x0_a), u2f(P0_mx_x0_b)); SB();
      const f32x4 ua02_k0 = mfma(Y20, WB00, Z4); SB();
      const u32x4 WB32_r = WFRAG(1, 3, 2); SB();
      const unsigned P0_mx_x1_c = vcopy(f2u(P0_mx_r)); SB();
      const f32x4 ua00_k1 = mfma(Y01, WB01, ua00_k0); SB();
      const float P1_t01 = S10[1] * Rr0[1]; SB();
      const auto P0_mx_x1 = __builtin_amdgcn_permlane32_swap(f2u(P0_mx_r), P0_mx_x1_c, false, false); const unsigned P0_mx_x1_a = P0_mx_x1[0], P0_mx_x1_b = P0_mx_x1[1]; SB();
      const f32x4 ua01_k1 = mfma(Y11, WB01, ua01_k0); SB();
      const float P0_mx = max2(u2f(P0_mx_x1_a), u2f(P0_mx_x1_b)); SB();
      const float P0_mn = -P0_mx * rc0; SB();
      const f32x4 ua02_k1 = mfma(Y21, WB01, ua02_k0); SB();
      auto WB10 = WB10_r; auto WB11 = WB11_r; auto WB12 = WB12_r; asm volatile("" : "+v"(WB10), "+v"(WB11), "+v"(WB12)); SB();
      const float P0_t00_z = fmaf(P0_t00, rc0, P0_mn); SB();
      const f32x4 ua00 = mfma(Y02, WB02, ua00_k1); SB();
      const float P0_t01_z = fmaf(P0_t01, rc0, P0_mn); SB();
      const float P0_t02_z = fmaf(P0_t02, rc0, P0_mn); SB();
      const f32x4 ua01 = mfma(Y12, WB02, ua01_k1); SB();
      const float P0_t03_z = fmaf(P0_t03, rc0, P0_mn); SB();
      const float P0_t10_z = fmaf(P0_t10, rc0, P0_mn); SB();
      const f32x4 ua02 = mfma(Y22, WB02, ua02_k1); SB();
      const float P0_t11_z = fmaf(P0_t11, rc0, P0_mn); SB();
      const float P0_t12_z = fmaf(P0_t12, rc0, P0_mn); SB();
      const f32x4 ua10_k0 = mfma(Y00, WB10, Z4); SB();
      const float rc2 = lds_ld<float>(r2c + 128); SB();
      const unsigned up00_0 = pk(ua00[0], ua00[1]); SB();
      const f32x4 ua11_k0 = mfma(Y10, WB10, Z4); SB();
      const unsigned up00_1 = pk(ua00[2], ua00[3]); SB();
      const unsigned up10_0 = pk(ua01[0], ua01[1]); SB();
      const f32x4 ua12_k0 = mfma(Y20, WB10, Z4); SB();
      const unsigned up10_1 = pk(ua01[2], ua01[3]); SB();
      const unsigned up20_0 = pk(ua02[0], ua02[1]); SB();
      const f32x4 ua10_k1 = mfma(Y01, WB11, ua10_k0); SB();
      const unsigned up20_1 = pk(ua02[2], ua02[3]); SB();
      const float P0_t13_z = fmaf(P0_t13, rc0, P0_mn); SB();
      const f32x4 ua11_k1 = mfma(Y11, WB11, ua11_k0); SB();
      const float P0_t20_z = fmaf(P0_t20, rc0, P0_mn); SB();
      const float P0_t21_z = fmaf(P0_t21, rc0, P0_mn); SB();
      const f32x4 ua12_k1 = mfma(Y21, WB11, ua12_k0); SB();
      auto WB20 = WB20_r; auto WB21 = WB21_r; auto WB22 = WB22_r; asm volatile("" : "+v"(WB20), "+v"(WB21), "+v"(WB22)); SB();
      const float P0_t22_z = fmaf(P0_t22, rc0, P0_mn); SB();
      const f32x4 ua10 = mfma(Y02, WB12, ua10_k1); SB();
      const float P0_t23_z = fmaf(P0_t23, rc0, P0_mn); SB();
      const float P0_t00_e = exp2_(P0_t00_z); SB();
      const f32x4 ua11 = mfma(Y12, WB12, ua11_k1); SB();
      const float P0_t01_e = exp2_(P0_t01_z); SB();
      const f32x4 ua12 = mfma(Y22, WB12, ua12_k1); SB();
      const float P0_t02_e = exp2_(P0_t02_z); SB();
      const f32x4 ua20_k0 = mfma(Y00, WB20, Z4); SB();
      const float P0_t03_e = exp2_(P0_t03_z); SB();
      const f32x4 ua21_k0 = mfma(Y10, WB20, Z4); SB();
      const unsigned up01_0 = pk(ua10[0], ua10[1]); SB();
      const unsigned up01_1 = pk(ua10[2], ua10[3]); SB();
      const f32x4 ua22_k0 = mfma(Y20, WB20, Z4); SB();
      const unsigned up11_0 = pk(ua11[0], ua11[1]); SB();
      const unsigned up11_1 = pk(ua11[2], ua11[3]); SB();
      const f32x4 ua20_k1 = mfma(Y01, WB21, ua20_k0); SB();
      const u32x4 WB40_r = WFRAG(1, 4, 0); SB();
      const unsigned up21_0 = pk(ua12[0], ua12[1]); SB();
      const f32x4 ua21_k1 = mfma(Y11, WB21, ua21_k0); SB();
      const u32x4 WB41_r = WFRAG(1, 4, 1); SB();
      const unsigned up21_1 = pk(ua12[2], ua12[3]); SB();
      const f32x4 ua22_k1 = mfma(Y21, WB21, ua22_k0); SB();
      auto WB30 = WB30_r; auto WB31 = WB31_r; auto WB32 = WB32_r; asm volatile("" : "+v"(WB30), "+v"(WB31), "+v"(WB32)); SB();
      const float P0_t10_e = exp2_(P0_t10_z); SB();
      const f32x4 ua20 = mfma(Y02, WB22, ua20_k1); SB();
      const u32x4 WB42_r = WFRAG(1, 4, 2); SB();
      const float P0_t11_e = exp2_(P0_t11_z); SB();
      const f32x4 ua21 = mfma(Y12, WB22, ua21_k1); SB();
      const float P0_t12_e = exp2_(P0_t12_z); SB();
      const f32x4 ua22 = mfma(Y22, WB22, ua22_k1); SB();
      const float P0_t13_e = exp2_(P0_t13_z); SB();
      const f32x4 ua30_k0 = mfma(Y00, WB30, Z4); SB();
      const float P0_t20_e = exp2_(P0_t20_z); SB();
      const f32x4 ua31_k0 = mfma(Y10, WB30, Z4); SB();
      const unsigned up02_0 = pk(ua20[0], ua20[1]); SB();
      const unsigned up02_1 = pk(ua20[2], ua20[3]); SB();
      const f32x4 ua32_k0 = mfma(Y20, WB30, Z4); SB();
      const unsigned up12_0 = pk(ua21[0], ua21[1]); SB();
      const unsigned up12_1 = pk(ua21[2], ua21[3]); SB();
      const f32x4 ua30_k1 = mfma(Y01, WB31, ua30_k0); SB();
      const u32x4 WB50_r = WFRAG(1, 5, 0); SB();
      const unsigned up22_0 = pk(ua22[0], ua22[1]); SB();
      const f32x4 ua31_k1 = mfma(Y11, WB31, ua31_k0); SB();
      const u32x4 WB51_r = WFRAG(1, 5, 1); SB();
      const unsigned up22_1 = pk(ua22[2], ua22[3]); SB();
      const f32x4 ua32_k1 = mfma(Y21, WB31, ua32_k0); SB();
      const u32x4 WB52_r = WFRAG(1, 5, 2); SB();
      const float P0_t21_e = exp2_(P0_t21_z); SB();
      const f32x4 ua30 = mfma(Y02, WB32, ua30_k1); SB();
      const float P0_t22_e = exp2_(P0_t22_z); SB();
      const f32x4 ua31 = mfma(Y12, WB32, ua31_k1); SB();
      const float P0_t23_e = exp2_(P0_t23_z); SB();
      const f32x4 ua32 = mfma(Y22, WB32, ua32_k1); SB();
      const float P0_tx_z = fmaf(P0_tx, rc0, P0_mn); SB();
      const float P0_l0 = P0_t00_e + P0_t01_e; SB();
      const f32x4 S20_k0 = mfma(Y00, u32x4{gp20_0, gp20_1, gp21_0, gp21_1}, Z4); SB();
      const float P0_l1 = P0_t02_e + P0_t03_e; SB();
      const unsigned up03_0 = pk(ua30[0], ua30[1]); SB();
      const f32x4 S21_k0 = mfma(Y10, u32x4{gp20_0, gp20_1, gp21_0, gp21_1}, Z4); SB();
      const unsigned up03_1 = pk(ua30[2], ua30[3]); SB();
      const unsigned up13_0 = pk(ua31[0], ua31[1]); SB();
      const f32x4 S22_k0 = mfma(Y20, u32x4{gp20_0, gp20_1, gp21_0, gp21_1}, Z4); SB();
      const unsigned up13_1 = pk(ua31[2], ua31[3]); SB();
      const unsigned up23_0 = pk(ua32[0], ua32[1]); SB();
      const f32x4 S23_k0 = mfma(XS0, u32x4{gp20_0, gp20_1, gp21_0, gp21_1}, Z4); SB();
      const u32x2 PS0_r = lds_ld<u32x2>(pscr + 0); SB();
      const unsigned up23_1 = pk(ua32[2], ua32[3]); SB();
      const f32x4 S20_k1 = mfma(Y01, u32x4{gp22_0, gp22_1, gp23_0, gp23_1}, S20_k0); SB();
      const u32x2 PS1_r = lds_ld<u32x2>(pscr + 32); SB();
      const float P0_l2 = P0_t10_e + P0_t11_e; SB();
      const f32x4 S21_k1 = mfma(Y11, u32x4{gp22_0, gp22_1, gp23_0, gp23_1}, S21_k0); SB();
      const u32x2 PS2_r = lds_ld<u32x2>(pscr + 64); SB();
      const float P0_l3 = P0_t12_e + P0_t13_e; SB();
      const f32x4 S22_k1 = mfma(Y21, u32x4{gp22_0, gp22_1, gp23_0, gp23_1}, S22_k0); SB();
      const u32x2 PS3_r = lds_ld<u32x2>(pscr + 96); SB();
      const float P0_l4 = P0_t20_e + P0_t21_e; SB();
      const f32x4 S23_k1 = mfma(XS1, u32x4{gp22_0, gp22_1, gp23_0, gp23_1}, S23_k0); SB();
      const unsigned US0 = lds_ld<unsigned short>(sua + 0); SB();
      const float P0_l5 = P0_t22_e + P0_t23_e; SB();
      const f32x4 S20 = mfma(Y02, u32x4{gp24_0, gp24_1, gp25_0, gp25_1}, S20_k1); SB();
      auto WB40 = WB40_r; auto WB41 = WB41_r; auto WB42 = WB42_r; asm volatile("" : "+v"(WB40), "+v"(WB41), "+v"(WB42)); SB();
      const float P0_tx_e = exp2_(P0_tx_z); SB();
      const f32x4 S21 = mfma(Y12, u32x4{gp24_0, gp24_1, gp25_0, gp25_1}, S21_k1); SB();
      const unsigned uz0 = lds_ld<unsigned short>(opaque(sua) + 0); SB();
      const float P0_l6 = P0_l0 + P0_l1; SB();
      const f32x4 S22 = mfma(Y22, u32x4{gp24_0, gp24_1, gp25_0, gp25_1}, S22_k1); SB();
      const unsigned US1 = lds_ld<unsigned short>(sua + 8); SB();
      const float P0_l7 = P0_l2 + P0_l3; SB();
      const f32x4 S23 = mfma(XS2, u32x4{gp24_0, gp24_1, gp25_0, gp25_1}, S23_k1); SB();
      const unsigned uz1 = lds_ld<unsigned short>(opaque(sua) + 8); SB();
      const float P0_l8 = P0_l4 + P0_l5; SB();
      const f32x4 ua40_k0 = mfma(Y00, WB40, Z4); SB();
      const unsigned US2 = lds_ld<unsigned short>(sua + 64); SB();
      const float P0_l9 = P0_l6 + P0_l7; SB();
      const f32x4 ua41_k0 = mfma(Y10, WB40, Z4); SB();
      const unsigned uz2 = lds_ld<unsigned short>(opaque(sua) + 64); SB();
      const float P0_l10 = P0_l8 + P0_tx_e; SB();
      const f32x4 ua42_k0 = mfma(Y20, WB40, Z4); SB();
      const unsigned US3 = lds_ld<unsigned short>(sua + 72); SB();
      const float P0_l11 = P0_l9 + P0_l10; SB();
      const f32x4 ua40_k1 = mfma(Y01, WB41, ua40_k0); SB();
      const unsigned uz3 = lds_ld<unsigned short>(opaque(sua) + 72); SB();
      const unsigned P0_ls_x0_c = vcopy(f2u(P0_l11)); SB();
      const f32x4 ua41_k1 = mfma(Y11, WB41, ua41_k0); SB();
      const float P0_t00_w = P0_t00_e * Rr0[0]; SB();
      const auto P0_ls_x0 = __builtin_amdgcn_permlane16_swap(f2u(P0_l11), P0_ls_x0_c, false, false); const unsigned P0_ls_x0_a = P0_ls_x0[0], P0_ls_x0_b = P0_ls_x0[1]; SB();
      const f32x4 ua42_k1 = mfma(Y21, WB41, ua42_k0); SB();
      auto WB50 = WB50_r; auto WB51 = WB51_r; auto WB52 = WB52_r; asm volatile("" : "+v"(WB50), "+v"(WB51), "+v"(WB52)); SB();
      const float P0_ls_r = u2f(P0_ls_x0_a) + u2f(P0_ls_x0_b); SB();
      const f32x4 ua40 = mfma(Y02, WB42, ua40_k1); SB();
      const unsigned US4 = lds_ld<unsigned short>(sua + 128); SB();
      const unsigned P0_ls_x1_c = vcopy(f2u(P0_ls_r)); SB();
      const f32x4 ua41 = mfma(Y12, WB42, ua41_k1); SB();
      const unsigned uz4 = lds_ld<unsigned short>(opaque(sua) + 128); SB();
      const auto P0_ls_x1 = __builtin_amdgcn_permlane32_swap(f2u(P0_ls_r), P0_ls_x1_c, false, false); const unsigned P0_ls_x1_a = P0_ls_x1[0], P0_ls_x1_b = P0_ls_x1[1]; SB();
      const f32x4 ua42 = mfma(Y22, WB42, ua42_k1); SB();
      const unsigned US5 = lds_ld<unsigned short>(sua + 136); SB();
      const float P0_ls = u2f(P0_ls_x1_a) + u2f(P0_ls_x1_b); SB();
      const f32x4 ua50_k0 = mfma(Y00, WB50, Z4); SB();
      const unsigned uz5 = lds_ld<unsigned short>(opaque(sua) + 136); SB();
      const unsigned up04_0 = pk(ua40[0], ua40[1]); SB();
      const f32x4 ua51_k0 = mfma(Y10, WB50, Z4); SB();
      const unsigned up04_1 = pk(ua40[2], ua40[3]); SB();
      const unsigned up14_0 = pk(ua41[0], ua41[1]); SB();
      const f32x4 ua52_k0 = mfma(Y20, WB50, Z4); SB();
      const unsigned up14_1 = pk(ua41[2], ua41[3]); SB();
      const unsigned up24_0 = pk(ua42[0], ua42[1]); SB();
      const f32x4 ua50_k1 = mfma(Y01, WB51, ua50_k0); SB();
      const unsigned up24_1 = pk(ua42[2], ua42[3]); SB();
      const float P0_i = __builtin_amdgcn_rcpf(P0_ls); SB();
      const f32x4 ua51_k1 = mfma(Y11, WB51, ua51_k0); SB();
      const float P0_t01_w = P0_t01_e * Rr0[1]; SB();
      const float P0_t02_w = P0_t02_e * Rr0[2]; SB();
      const f32x4 ua52_k1 = mfma(Y21, WB51, ua52_k0); SB();
      auto PS0 = PS0_r; auto PS1 = PS1_r; auto PS2 = PS2_r; auto PS3 = PS3_r; asm volatile("" : "+v"(PS0), "+v"(PS1), "+v"(PS2), "+v"(PS3)); SB();
      const float P0_t03_w = P0_t03_e * Rr0[3]; SB();
      const f32x4 ua50 = mfma(Y02, WB52, ua50_k1); SB();
      const float P0_t10_w = P0_t10_e * Rr1[0]; SB();
      const float P0_t11_w = P0_t11_e * Rr1[1]; SB();
      const f32x4 ua51 = mfma(Y12, WB52, ua51_k1); SB();
      const float P0_t12_w = P0_t12_e * Rr1[2]; SB();
      const float P0_t13_w = P0_t13_e * Rr1[3]; SB();
      const f32x4 ua52 = mfma(Y22, WB52, ua52_k1); SB();
      const float P0_t00_p = P0_t00_w * P0_i; SB();
      const float P0_t01_p = P0_t01_w * P0_i; SB();
      const f32x4 ox0_a = mfma(u32x4{up00_0, up00_1, up10_0, up10_1}, u32x4{PS0[0], PS0[1], PS1[0], PS1[1]}, Z4); SB();
      const unsigned up05_0 = pk(ua50[0], ua50[1]); SB();
      const unsigned up05_1 = pk(ua50[2], ua50[3]); SB();
      const f32x4 ox1_a = mfma(u32x4{up01_0, up01_1, up11_0, up11_1}, u32x4{PS0[0], PS0[1], PS1[0], PS1[1]}, Z4); SB();
      const unsigned up15_0 = pk(ua51[0], ua51[1]); SB();
      const unsigned up15_1 = pk(ua51[2], ua51[3]); SB();
      const f32x4 ox0_b = mfma(u32x4{up20_0, up20_1, US0, uz0}, u32x4{PS2[0], PS2[1], PS3[0], PS3[1]}, ox0_a); SB();
      const unsigned up25_0 = pk(ua52[0], ua52[1]); SB();
      const unsigned up25_1 = pk(ua52[2], ua52[3]); SB();
      const f32x4 ox1_b = mfma(u32x4{up21_0, up21_1, US1, uz1}, u32x4{PS2[0], PS2[1], PS3[0], PS3[1]}, ox1_a); SB();
      const float P0_t02_p = P0_t02_w * P0_i; SB();
      const float P0_t03_p = P0_t03_w * P0_i; SB();
      const f32x4 ox0 = mfma(SEL0, XS0, ox0_b); SB();
      const float P0_t10_p = P0_t10_w * P0_i; SB();
      const float P0_t11_p = P0_t11_w * P0_i; SB();
      const f32x4 ox1 = mfma(SEL1, XS0, ox1_b); SB();
      const float P0_t12_p = P0_t12_w * P0_i; SB();
      const float P0_t13_p = P0_t13_w * P0_i; SB();
      const f32x4 ox2_a = mfma(u32x4{up02_0, up02_1, up12_0, up12_1}, u32x4{PS0[0], PS0[1], PS1[0], PS1[1]}, Z4); SB();
      const unsigned P0_0 = pk(P0_t00_p, P0_t01_p); SB();
      const unsigned P0_1 = pk(P0_t02_p, P0_t03_p); SB();
      const f32x4 ox3_a = mfma(u32x4{up03_0, up03_1, up13_0, up13_1}, u32x4{PS0[0], PS0[1], PS1[0], PS1[1]}, Z4); SB();
      const unsigned kx0_0 = pk(ox0[0], ox0[1]); SB();
      const unsigned kx0_1 = pk(ox0[2], ox0[3]); SB();
      const f32x4 ox2_b = mfma(u32x4{up22_0, up22_1, US2, uz2}, u32x4{PS2[0], PS2[1], PS3[0], PS3[1]}, ox2_a); SB();
      const unsigned kx1_0 = pk(ox1[0], ox1[1]); SB();
      const unsigned kx1_1 = pk(ox1[2], ox1[3]); SB();
      const f32x4 ox3_b = mfma(u32x4{up23_0, up23_1, US3, uz3}, u32x4{PS2[0], PS2[1], PS3[0], PS3[1]}, ox3_a); SB();
      bstore(ors, u32x4{kx0_0, kx0_1, kx1_0, kx1_1}, sox + 0); SB();
      const f32x4 ox2 = mfma(SEL0, XS1, ox2_b); SB();
      const unsigned P0_2 = pk(P0_t10_p, P0_t11_p); SB();
      const unsigned P0_3 = pk(P0_t12_p, P0_t13_p); SB();
      const f32x4 ox3 = mfma(SEL1, XS1, ox3_b); SB();
      const float P0_tx_w = P0_tx_e * rs; SB();
      const float P0_t20_w = P0_t20_e * Rr2[0]; SB();
      const f32x4 ox4_a = mfma(u32x4{up04_0, up04_1, up14_0, up14_1}, u32x4{PS0[0], PS0[1], PS1[0], PS1[1]}, Z4); SB();
      const float P0_t21_w = P0_t21_e * Rr2[1]; SB();
      const float P0_t22_w = P0_t22_e * Rr2[2]; SB();
      const f32x4 ox5_a = mfma(u32x4{up05_0, up05_1, up15_0, up15_1}, u32x4{PS0[0], PS0[1], PS1[0], PS1[1]}, Z4); SB();
      const unsigned kx2_0 = pk(ox2[0], ox2[1]); SB();
      const unsigned kx2_1 = pk(ox2[2], ox2[3]); SB();
      const f32x4 ox4_b = mfma(u32x4{up24_0, up24_1, US4, uz4}, u32x4{PS2[0], PS2[1], PS3[0], PS3[1]}, ox4_a); SB();
      const unsigned kx3_0 = pk(ox3[0], ox3[1]); SB();
      const unsigned kx3_1 = pk(ox3[2], ox3[3]); SB();
      const f32x4 ox5_b = mfma(u32x4{up25_0, up25_1, US5, uz5}, u32x4{PS2[0], PS2[1], PS3[0], PS3[1]}, ox5_a); SB();
      bstore(ors, u32x4{kx2_0, kx2_1, kx3_0, kx3_1}, sox + 64); SB();
      const f32x4 ox4 = mfma(SEL0, XS2, ox4_b); SB();
      const float P0_t23_w = P0_t23_e * Rr2[3]; SB();
      const float P0_tx_p = P0_tx_w * P0_i; SB();
      const f32x4 ox5 = mfma(SEL1, XS2, ox5_b); SB();
      const float P0_t20_p = P0_t20_w * P0_i; SB();
      const float P0_t21_p = P0_t21_w * P0_i; SB();
      const f32x4 o00_a = mfma(u32x4{up00_0, up00_1, up10_0, up10_1}, u32x4{P0_0, P0_1, P0_2, P0_3}, Z4); SB();
      const float P0_t22_p = P0_t22_w * P0_i; SB();
      const float P0_t23_p = P0_t23_w * P0_i; SB();
      const f32x4 o01_a = mfma(u32x4{up01_0, up01_1, up11_0, up11_1}, u32x4{P0_0, P0_1, P0_2, P0_3}, Z4); SB();
      const unsigned P0_6 = pk(P0_tx_p, 0.f); SB();
      const unsigned P0_4 = pk(P0_t20_p, P0_t21_p); SB();
      const f32x4 o02_a = mfma(u32x4{up02_0, up02_1, up12_0, up12_1}, u32x4{P0_0, P0_1, P0_2, P0_3}, Z4); SB();
      const unsigned P0_5 = pk(P0_t22_p, P0_t23_p); SB();
      const unsigned P0_7 = vcopy(0u); SB();
      const f32x4 o03_a = mfma(u32x4{up03_0, up03_1, up13_0, up13_1}, u32x4{P0_0, P0_1, P0_2, P0_3}, Z4); SB();
      const unsigned kx4_0 = pk(ox4[0], ox4[1]); SB();
      const unsigned kx4_1 = pk(ox4[2], ox4[3]); SB();
      const f32x4 o00_b = mfma(u32x4{up20_0, up20_1, US0, uz0}, u32x4{P0_4, P0_5, P0_6, P0_7}, o00_a); SB();
      const unsigned kx5_0 = pk(ox5[0], ox5[1]); SB();
      const unsigned kx5_1 = pk(ox5[2], ox5[3]); SB();
      const f32x4 o01_b = mfma(u32x4{up21_0, up21_1, US1, uz1}, u32x4{P0_4, P0_5, P0_6, P0_7}, o01_a); SB();
      bstore(ors, u32x4{kx4_0, kx4_1, kx5_0, kx5_1}, sox + 128); SB();
      const f32x4 o00 = mfma(SEL0, Y00, o00_b); SB();
      const float P1_t02 = S10[2] * Rr0[2]; SB();
      const float P1_t03 = S10[3] * Rr0[3]; SB();
      const f32x4 o01 = mfma(SEL1, Y00, o01_b); SB();
      const float P1_t10 = S11[0] * Rr1[0]; SB();
      const float P1_t11 = S11[1] * Rr1[1]; SB();
      const f32x4 o02_b = mfma(u32x4{up22_0, up22_1, US2, uz2}, u32x4{P0_4, P0_5, P0_6, P0_7}, o02_a); SB();
      const float P1_t12 = S11[2] * Rr1[2]; SB();
      const float P1_t13 = S11[3] * Rr1[3]; SB();
      const f32x4 o03_b = mfma(u32x4{up23_0, up23_1, US3, uz3}, u32x4{P0_4, P0_5, P0_6, P0_7}, o03_a); SB();
      const unsigned k00_0 = pk(o00[0], o00[1]); SB();
      const unsigned k00_1 = pk(o00[2], o00[3]); SB();
      const f32x4 o02 = mfma(SEL0, Y01, o02_b); SB();
      const unsigned k01_0 = pk(o01[0], o01[1]); SB();
      const unsigned k01_1 = pk(o01[2], o01[3]); SB();
      const f32x4 o03 = mfma(SEL1, Y01, o03_b); SB();
      bstore(ors, u32x4{k00_0, k00_1, k01_0, k01_1}, so0 + 0); SB();
      const f32x4 o04_a = mfma(u32x4{up04_0, up04_1, up14_0, up14_1}, u32x4{P0_0, P0_1, P0_2, P0_3}, Z4); SB();
      const float P1_t20 = S12[0] * Rr2[0]; SB();
      const float P1_t21 = S12[1] * Rr2[1]; SB();
      const f32x4 o05_a = mfma(u32x4{up05_0, up05_1, up15_0, up15_1}, u32x4{P0_0, P0_1, P0_2, P0_3}, Z4); SB();
      const float P1_t22 = S12[2] * Rr2[2]; SB();
      const float P1_t23 = S12[3] * Rr2[3]; SB();
      const f32x4 o04_b = mfma(u32x4{up24_0, up24_1, US4, uz4}, u32x4{P0_4, P0_5, P0_6, P0_7}, o04_a); SB();
      const float P1_m0 = max3(P1_t00, P1_t01, P1_t02); SB();
      const float P1_m1 = max3(P1_t03, P1_t10, P1_t11); SB();
      const f32x4 o05_b = mfma(u32x4{up25_0, up25_1, US5, uz5}, u32x4{P0_4, P0_5, P0_6, P0_7}, o05_a); SB();
      const float P1_m2 = max3(P1_t12, P1_t13, P1_t20); SB();
      const float P1_tx = fmaf(S13[0], rs, biask); KEEP(S13); SB();
      const f32x4 o04 = mfma(SEL0, Y02, o04_b); SB();
      const float P1_m3 = max3(P1_t21, P1_t22, P1_t23); SB();
      const float P1_m4 = max3(P1_m0, P1_m1, P1_m2); SB();
      const f32x4 o05 = mfma(SEL1, Y02, o05_b); SB();
      const float P1_m5 = max3(P1_m4, P1_m3, P1_tx); SB();
      const unsigned P1_mx_x0_c = vcopy(f2u(P1_m5)); SB();
      const unsigned k02_0 = pk(o02[0], o02[1]); SB();
      const unsigned k02_1 = pk(o02[2], o02[3]); SB();
      const auto P1_mx_x0 = __builtin_amdgcn_permlane16_swap(f2u(P1_m5), P1_mx_x0_c, false, false); const unsigned P1_mx_x0_a = P1_mx_x0[0], P1_mx_x0_b = P1_mx_x0[1]; SB();
      const float P1_mx_r = max2(u2f(P1_mx_x0_a), u2f(P1_mx_x0_b)); SB();
      const unsigned P1_mx_x1_c = vcopy(f2u(P1_mx_r)); SB();
      const unsigned k03_0 = pk(o03[0], o03[1]); SB();
      const unsigned k03_1 = pk(o03[2], o03[3]); SB();
      const auto P1_mx_x1 = __builtin_amdgcn_permlane32_swap(f2u(P1_mx_r), P1_mx_x1_c, false, false); const unsigned P1_mx_x1_a = P1_mx_x1[0], P1_mx_x1_b = P1_mx_x1[1]; SB();
      const float P1_mx = max2(u2f(P1_mx_x1_a), u2f(P1_mx_x1_b)); SB();
      const float P1_mn = -P1_mx * rc1; SB();
      const float P1_t00_z = fmaf(P1_t00, rc1, P1_mn); SB();
      const float P1_t01_z = fmaf(P1_t01, rc1, P1_mn); SB();
      const float P1_t02_z = fmaf(P1_t02, rc1, P1_mn); SB();
      const float P1_t03_z = fmaf(P1_t03, rc1, P1_mn); SB();
      const float P1_t10_z = fmaf(P1_t10, rc1, P1_mn); SB();
      const float P1_t11_z = fmaf(P1_t11, rc1, P1_mn); SB();
      const float P1_t12_z = fmaf(P1_t12, rc1, P1_mn); SB();
      const float P1_t13_z = fmaf(P1_t13, rc1, P1_mn); SB();
      const float P1_t20_z = fmaf(P1_t20, rc1, P1_mn); SB();
      const float P1_t21_z = fmaf(P1_t21, rc1, P1_mn); SB();
      const float P1_t22_z = fmaf(P1_t22, rc1, P1_mn); SB();
      const float P1_t23_z = fmaf(P1_t23, rc1, P1_mn); SB();
      const float P1_t00_e = exp2_(P1_t00_z); SB();
      const float P1_t01_e = exp2_(P1_t01_z); SB();
      const float P1_t02_e = exp2_(P1_t02_z); SB();
      const float P1_t03_e = exp2_(P1_t03_z); SB();
      const float P1_t10_e = exp2_(P1_t10_z); SB();
      const float P1_t11_e = exp2_(P1_t11_z); SB();
      const float P1_t12_e = exp2_(P1_t12_z); SB();
      const float P1_t13_e = exp2_(P1_t13_z); SB();
      const float P1_t20_e = exp2_(P1_t20_z); SB();
      const float P1_t21_e = exp2_(P1_t21_z); SB();
      const float P1_t22_e = exp2_(P1_t22_z); SB();
      const float P1_t23_e = exp2_(P1_t23_z); SB();
      const float P1_tx_z = fmaf(P1_tx, rc1, P1_mn); SB();
      const float P1_l0 = P1_t00_e + P1_t01_e; SB();
      const float P1_l1 = P1_t02_e + P1_t03_e; SB();
      const float P1_l2 = P1_t10_e + P1_t11_e; SB();
      const float P1_l3 = P1_t12_e + P1_t13_e; SB();
      const float P1_l4 = P1_t20_e + P1_t21_e; SB();
      const float P1_l5 = P1_t22_e + P1_t23_e; SB();
      const float P1_tx_e = exp2_(P1_tx_z); SB();
      const float P1_l6 = P1_l0 + P1_l1; SB();
      const float P1_l7 = P1_l2 + P1_l3; SB();
      const float P1_l8 = P1_l4 + P1_l5; SB();
      const float P1_l9 = P1_l6 + P1_l7; SB();
      const float P1_l10 = P1_l8 + P1_tx_e; SB();
      const float P1_l11 = P1_l9 + P1_l10; SB();
      const unsigned P1_ls_x0_c = vcopy(f2u(P1_l11)); SB();
      const float P1_t00_w = P1_t00_e * Rr0[0]; SB();
      const float P1_t01_w = P1_t01_e * Rr0[1]; SB();
      const auto P1_ls_x0 = __builtin_amdgcn_permlane16_swap(f2u(P1_l11), P1_ls_x0_c, false, false); const unsigned P1_ls_x0_a = P1_ls_x0[0], P1_ls_x0_b = P1_ls_x0[1]; SB();
      const float P1_ls_r = u2f(P1_ls_x0_a) + u2f(P1_ls_x0_b); SB();
      const unsigned P1_ls_x1_c = vcopy(f2u(P1_ls_r)); SB();
      const float P1_t02_w = P1_t02_e * Rr0[2]; SB();
      const float P1_t03_w = P1_t03_e * Rr0[3]; SB();
      const auto P1_ls_x1 = __builtin_amdgcn_permlane32_swap(f2u(P1_ls_r), P1_ls_x1_c, false, false); const unsigned P1_ls_x1_a = P1_ls_x1[0], P1_ls_x1_b = P1_ls_x1[1]; SB();
      const float P1_ls = u2f(P1_ls_x1_a) + u2f(P1_ls_x1_b); SB();
      const float P1_i = __builtin_amdgcn_rcpf(P1_ls); SB();
      const float P1_t10_w = P1_t10_e * Rr1[0]; SB();
      const float P1_t11_w = P1_t11_e * Rr1[1]; SB();
      const float P1_t12_w = P1_t12_e * Rr1[2]; SB();
      const float P1_t13_w = P1_t13_e * Rr1[3]; SB();
      const float P1_t00_p = P1_t00_w * P1_i; SB();
      const float P1_t01_p = P1_t01_w * P1_i; SB();
      const float P1_t02_p = P1_t02_w * P1_i; SB();
      const float P1_t03_p = P1_t03_w * P1_i; SB();
      const float P1_t10_p = P1_t10_w * P1_i; SB();
      const float P1_t11_p = P1_t11_w * P1_i; SB();
      const float P1_t12_p = P1_t12_w * P1_i; SB();
      const float P1_t13_p = P1_t13_w * P1_i; SB();
      const unsigned P1_0 = pk(P1_t00_p, P1_t01_p); SB();
      const unsigned P1_1 = pk(P1_t02_p, P1_t03_p); SB();
      const unsigned P1_2 = pk(P1_t10_p, P1_t11_p); SB();
      const unsigned P1_3 = pk(P1_t12_p, P1_t13_p); SB();
      const float P1_tx_w = P1_tx_e * rs; SB();
      const float P1_t20_w = P1_t20_e * Rr2[0]; SB();
      const f32x4 o10_a = mfma(u32x4{up00_0, up00_1, up10_0, up10_1}, u32x4{P1_0, P1_1, P1_2, P1_3}, Z4); SB();
      bstore(ors, u32x4{k02_0, k02_1, k03_0, k03_1}, so0 + 64); SB();
      const f32x4 o11_a = mfma(u32x4{up01_0, up01_1, up11_0, up11_1}, u32x4{P1_0, P1_1, P1_2, P1_3}, Z4); SB();
      const float P1_t21_w = P1_t21_e * Rr2[1]; SB();
      const float P1_t22_w = P1_t22_e * Rr2[2]; SB();
      const f32x4 o12_a = mfma(u32x4{up02_0, up02_1, up12_0, up12_1}, u32x4{P1_0, P1_1, P1_2, P1_3}, Z4); SB();
      const float P1_t23_w = P1_t23_e * Rr2[3]; SB();
      const float P1_tx_p = P1_tx_w * P1_i; SB();
      const f32x4 o13_a = mfma(u32x4{up03_0, up03_1, up13_0, up13_1}, u32x4{P1_0, P1_1, P1_2, P1_3}, Z4); SB();
      const float P1_t20_p = P1_t20_w * P1_i; SB();
      const float P1_t21_p = P1_t21_w * P1_i; SB();
      const float P1_t22_p = P1_t22_w * P1_i; SB();
      const float P1_t23_p = P1_t23_w * P1_i; SB();
      const unsigned P1_6 = pk(P1_tx_p, 0.f); SB();
      const unsigned P1_4 = pk(P1_t20_p, P1_t21_p); SB();
      const unsigned P1_5 = pk(P1_t22_p, P1_t23_p); SB();
      const unsigned P1_7 = vcopy(0u); SB();
      const unsigned k04_0 = pk(o04[0], o04[1]); SB();
      const unsigned k04_1 = pk(o04[2], o04[3]); SB();
      const f32x4 o10_b = mfma(u32x4{up20_0, up20_1, US0, uz0}, u32x4{P1_4, P1_5, P1_6, P1_7}, o10_a); SB();
      const unsigned k05_0 = pk(o05[0], o05[1]); SB();
      const unsigned k05_1 = pk(o05[2], o05[3]); SB();
      const f32x4 o11_b = mfma(u32x4{up21_0, up21_1, US1, uz1}, u32x4{P1_4, P1_5, P1_6, P1_7}, o11_a); SB();
      bstore(ors, u32x4{k04_0, k04_1, k05_0, k05_1}, so0 + 128); SB();
      const f32x4 o10 = mfma(SEL0, Y10, o10_b); SB();
      const float P2_t00 = S20[0] * Rr0[0]; SB();
      const float P2_t01 = S20[1] * Rr0[1]; SB();
      const f32x4 o11 = mfma(SEL1, Y10, o11_b); SB();
      const float P2_t02 = S20[2] * Rr0[2]; SB();
      const float P2_t03 = S20[3] * Rr0[3]; SB();
      const f32x4 o12_b = mfma(u32x4{up22_0, up22_1, US2, uz2}, u32x4{P1_4, P1_5, P1_6, P1_7}, o12_a); SB();
      const float P2_t10 = S21[0] * Rr1[0]; SB();
      const float P2_t11 = S21[1] * Rr1[1]; SB();
      const f32x4 o13_b = mfma(u32x4{up23_0, up23_1, US3, uz3}, u32x4{P1_4, P1_5, P1_6, P1_7}, o13_a); SB();
      const unsigned k10_0 = pk(o10[0], o10[1]); SB();
      const unsigned k10_1 = pk(o10[2], o10[3]); SB();
      const f32x4 o12 = mfma(SEL0, Y11, o12_b); SB();
      const unsigned k11_0 = pk(o11[0], o11[1]); SB();
      const unsigned k11_1 = pk(o11[2], o11[3]); SB();
      const f32x4 o13 = mfma(SEL1, Y11, o13_b); SB();
      bstore(ors, u32x4{k10_0, k10_1, k11_0, k11_1}, so1 + 0); SB();
      const f32x4 o14_a = mfma(u32x4{up04_0, up04_1, up14_0, up14_1}, u32x4{P1_0, P1_1, P1_2, P1_3}, Z4); SB();
      const float P2_t12 = S21[2] * Rr1[2]; SB();
      const float P2_t13 = S21[3] * Rr1[3]; SB();
      const f32x4 o15_a = mfma(u32x4{up05_0, up05_1, up15_0, up15_1}, u32x4{P1_0, P1_1, P1_2, P1_3}, Z4); SB();
      const float P2_t20 = S22[0] * Rr2[0]; SB();
      const float P2_t21 = S22[1] * Rr2[1]; SB();
      const f32x4 o14_b = mfma(u32x4{up24_0, up24_1, US4, uz4}, u32x4{P1_4, P1_5, P1_6, P1_7}, o14_a); SB();
      const float P2_t22 = S22[2] * Rr2[2]; SB();
      const float P2_t23 = S22[3] * Rr2[3]; SB();
      const f32x4 o15_b = mfma(u32x4{up25_0, up25_1, US5, uz5}, u32x4{P1_4, P1_5, P1_6, P1_7}, o15_a); SB();
      const float P2_m0 = max3(P2_t00, P2_t01, P2_t02); SB();
      const float P2_m1 = max3(P2_t03, P2_t10, P2_t11); SB();
      const f32x4 o14 = mfma(SEL0, Y12, o14_b); SB();
      const float P2_m2 = max3(P2_t12, P2_t13, P2_t20); SB();
      const float P2_tx = fmaf(S23[0], rs, biask); KEEP(S23); SB();
      const f32x4 o15 = mfma(SEL1, Y12, o15_b); SB();
      const float P2_m3 = max3(P2_t21, P2_t22, P2_t23); SB();
      const float P2_m4 = max3(P2_m0, P2_m1, P2_m2); SB();
      const float P2_m5 = max3(P2_m4, P2_m3, P2_tx); SB();
      const unsigned P2_mx_x0_c = vcopy(f2u(P2_m5)); SB();
      const unsigned k12_0 = pk(o12[0], o12[1]); SB();
      const unsigned k12_1 = pk(o12[2], o12[3]); SB();
      const auto P2_mx_x0 = __builtin_amdgcn_permlane16_swap(f2u(P2_m5), P2_mx_x0_c, false, false); const unsigned P2_mx_x0_a = P2_mx_x0[0], P2_mx_x0_b = P2_mx_x0[1]; SB();
      const float P2_mx_r = max2(u2f(P2_mx_x0_a), u2f(P2_mx_x0_b)); SB();
      const unsigned P2_mx_x1_c = vcopy(f2u(P2_mx_r)); SB();
      const unsigned k13_0 = pk(o13[0], o13[1]); SB();
      const unsigned k13_1 = pk(o13[2], o13[3]); SB();
      const auto P2_mx_x1 = __builtin_amdgcn_permlane32_swap(f2u(P2_mx_r), P2_mx_x1_c, false, false); const unsigned P2_mx_x1_a = P2_mx_x1[0], P2_mx_x1_b = P2_mx_x1[1]; SB();
      const float P2_mx = max2(u2f(P2_mx_x1_a), u2f(P2_mx_x1_b)); SB();
      const float P2_mn = -P2_mx * rc2; SB();
      const float P2_t00_z = fmaf(P2_t00, rc2, P2_mn); SB();
      const float P2_t01_z = fmaf(P2_t01, rc2, P2_mn); SB();
      const float P2_t02_z = fmaf(P2_t02, rc2, P2_mn); SB();
      const float P2_t03_z = fmaf(P2_t03, rc2, P2_mn); SB();
      const float P2_t10_z = fmaf(P2_t10, rc2, P2_mn); SB();
      const float P2_t11_z = fmaf(P2_t11, rc2, P2_mn); SB();
      const float P2_t12_z = fmaf(P2_t12, rc2, P2_mn); SB();
      const float P2_t13_z = fmaf(P2_t13, rc2, P2_mn); SB();
      const float P2_t20_z = fmaf(P2_t20, rc2, P2_mn); SB();
      const float P2_t21_z = fmaf(P2_t21, rc2, P2_mn); SB();
      const float P2_t22_z = fmaf(P2_t22, rc2, P2_mn); SB();
      const float P2_t23_z = fmaf(P2_t23, rc2, P2_mn); SB();
      const float P2_t00_e = exp2_(P2_t00_z); SB();
      const float P2_t01_e = exp2_(P2_t01_z); SB();
      const float P2_t02_e = exp2_(P2_t02_z); SB();
      const float P2_t03_e = exp2_(P2_t03_z); SB();
      const float P2_t10_e = exp2_(P2_t10_z); SB();
      const float P2_t11_e = exp2_(P2_t11_z); SB();
      const float P2_t12_e = exp2_(P2_t12_z); SB();
      const float P2_t13_e = exp2_(P2_t13_z); SB();
      const float P2_t20_e = exp2_(P2_t20_z); SB();
      const float P2_t21_e = exp2_(P2_t21_z); SB();
      const float P2_t22_e = exp2_(P2_t22_z); SB();
      const float P2_t23_e = exp2_(P2_t23_z); SB();
      const float P2_tx_z = fmaf(P2_tx, rc2, P2_mn); SB();
      const float P2_l0 = P2_t00_e + P2_t01_e; SB();
      const float P2_l1 = P2_t02_e + P2_t03_e; SB();
      const float P2_l2 = P2_t10_e + P2_t11_e; SB();
      const float P2_l3 = P2_t12_e + P2_t13_e; SB();
      const float P2_l4 = P2_t20_e + P2_t21_e; SB();
      const float P2_l5 = P2_t22_e + P2_t23_e; SB();
      const float P2_tx_e = exp2_(P2_tx_z); SB();
      const float P2_l6 = P2_l0 + P2_l1; SB();
      const float P2_l7 = P2_l2 + P2_l3; SB();
      const float P2_l8 = P2_l4 + P2_l5; SB();
      const float P2_l9 = P2_l6 + P2_l7; SB();
      const float P2_l10 = P2_l8 + P2_tx_e; SB();
      const float P2_l11 = P2_l9 + P2_l10; SB();
      const unsigned P2_ls_x0_c = vcopy(f2u(P2_l11)); SB();
      const float P2_t00_w = P2_t00_e * Rr0[0]; SB();
      const float P2_t01_w = P2_t01_e * Rr0[1]; SB();
      const auto P2_ls_x0 = __builtin_amdgcn_permlane16_swap(f2u(P2_l11), P2_ls_x0_c, false, false); const unsigned P2_ls_x0_a = P2_ls_x0[0], P2_ls_x0_b = P2_ls_x0[1]; SB();
      const float P2_ls_r = u2f(P2_ls_x0_a) + u2f(P2_ls_x0_b); SB();
      const unsigned P2_ls_x1_c = vcopy(f2u(P2_ls_r)); SB();
      const float P2_t02_w = P2_t02_e * Rr0[2]; SB();
      const float P2_t03_w = P2_t03_e * Rr0[3]; SB();
      const auto P2_ls_x1 = __builtin_amdgcn_permlane32_swap(f2u(P2_ls_r), P2_ls_x1_c, false, false); const unsigned P2_ls_x1_a = P2_ls_x1[0], P2_ls_x1_b = P2_ls_x1[1]; SB();
      const float P2_ls = u2f(P2_ls_x1_a) + u2f(P2_ls_x1_b); SB();
      const float P2_i = __builtin_amdgcn_rcpf(P2_ls); SB();
      const float P2_t10_w = P2_t10_e * Rr1[0]; SB();
      const float P2_t11_w = P2_t11_e * Rr1[1]; SB();
      const float P2_t12_w = P2_t12_e * Rr1[2]; SB();
      const float P2_t13_w = P2_t13_e * Rr1[3]; SB();
      const float P2_t00_p = P2_t00_w * P2_i; SB();
      const float P2_t01_p = P2_t01_w * P2_i; SB();
      const float P2_t02_p = P2_t02_w * P2_i; SB();
      const float P2_t03_p = P2_t03_w * P2_i; SB();
      const float P2_t10_p = P2_t10_w * P2_i; SB();
      const float P2_t11_p = P2_t11_w * P2_i; SB();
      const float P2_t12_p = P2_t12_w * P2_i; SB();
      const float P2_t13_p = P2_t13_w * P2_i; SB();
      const unsigned P2_0 = pk(P2_t00_p, P2_t01_p); SB();
      const unsigned P2_1 = pk(P2_t02_p, P2_t03_p); SB();
      const unsigned P2_2 = pk(P2_t10_p, P2_t11_p); SB();
      const unsigned P2_3 = pk(P2_t12_p, P2_t13_p); SB();
      const float P2_tx_w = P2_tx_e * rs; SB();
      const float P2_t20_w = P2_t20_e * Rr2[0]; SB();
      const f32x4 o20_a = mfma(u32x4{up00_0, up00_1, up10_0, up10_1}, u32x4{P2_0, P2_1, P2_2, P2_3}, Z4); SB();
      bstore(ors, u32x4{k12_0, k12_1, k13_0, k13_1}, so1 + 64); SB();
      const f32x4 o21_a = mfma(u32x4{up01_0, up01_1, up11_0, up11_1}, u32x4{P2_0, P2_1, P2_2, P2_3}, Z4); SB();
      const float P2_t21_w = P2_t21_e * Rr2[1]; SB();
      const float P2_t22_w = P2_t22_e * Rr2[2]; SB();
      const f32x4 o22_a = mfma(u32x4{up02_0, up02_1, up12_0, up12_1}, u32x4{P2_0, P2_1, P2_2, P2_3}, Z4); SB();
      const float P2_t23_w = P2_t23_e * Rr2[3]; SB();
      const float P2_tx_p = P2_tx_w * P2_i; SB();
      const f32x4 o23_a = mfma(u32x4{up03_0, up03_1, up13_0, up13_1}, u32x4{P2_0, P2_1, P2_2, P2_3}, Z4); SB();
      const float P2_t20_p = P2_t20_w * P2_i; SB();
      const float P2_t21_p = P2_t21_w * P2_i; SB();
      const float P2_t22_p = P2_t22_w * P2_i; SB();
      const float P2_t23_p = P2_t23_w * P2_i; SB();
      const unsigned P2_6 = pk(P2_tx_p, 0.f); SB();
      const unsigned P2_4 = pk(P2_t20_p, P2_t21_p); SB();
      const unsigned P2_5 = pk(P2_t22_p, P2_t23_p); SB();
      const unsigned P2_7 = vcopy(0u); SB();
      const unsigned k14_0 = pk(o14[0], o14[1]); SB();
      const unsigned k14_1 = pk(o14[2], o14[3]); SB();
      const f32x4 o20_b = mfma(u32x4{up20_0, up20_1, US0, uz0}, u32x4{P2_4, P2_5, P2_6, P2_7}, o20_a); SB();
      const unsigned k15_0 = pk(o15[0], o15[1]); SB();
      const unsigned k15_1 = pk(o15[2], o15[3]); SB();
      const f32x4 o21_b = mfma(u32x4{up21_0, up21_1, US1, uz1}, u32x4{P2_4, P2_5, P2_6, P2_7}, o21_a); SB();
      bstore(ors, u32x4{k14_0, k14_1, k15_0, k15_1}, so1 + 128); SB();
      const f32x4 o20 = mfma(SEL0, Y20, o20_b); SB();
      const f32x4 o21 = mfma(SEL1, Y20, o21_b); SB();
      const f32x4 o22_b = mfma(u32x4{up22_0, up22_1, US2, uz2}, u32x4{P2_4, P2_5, P2_6, P2_7}, o22_a); SB();
      const f32x4 o23_b = mfma(u32x4{up23_0, up23_1, US3, uz3}, u32x4{P2_4, P2_5, P2_6, P2_7}, o23_a); SB();
      const f32x4 o22 = mfma(SEL0, Y21, o22_b); SB();
      const f32x4 o23 = mfma(SEL1, Y21, o23_b); SB();
      const f32x4 o24_a = mfma(u32x4{up04_0, up04_1, up14_0, up14_1}, u32x4{P2_0, P2_1, P2_2, P2_3}, Z4); SB();
      const f32x4 o25_a = mfma(u32x4{up05_0, up05_1, up15_0, up15_1}, u32x4{P2_0, P2_1, P2_2, P2_3}, Z4); SB();
      const f32x4 o24_b = mfma(u32x4{up24_0, up24_1, US4, uz4}, u32x4{P2_4, P2_5, P2_6, P2_7}, o24_a); SB();
      const unsigned k20_0 = pk(o20[0], o20[1]); SB();
      const unsigned k20_1 = pk(o20[2], o20[3]); SB();
      const f32x4 o25_b = mfma(u32x4{up25_0, up25_1, US5, uz5}, u32x4{P2_4, P2_5, P2_6, P2_7}, o25_a); SB();
      const unsigned k21_0 = pk(o21[0], o21[1]); SB();
      const unsigned k21_1 = pk(o21[2], o21[3]); SB();
      const f32x4 o24 = mfma(SEL0, Y22, o24_b); SB();
      bstore(ors, u32x4{k20_0, k20_1, k21_0, k21_1}, so2 + 0); SB();
      const f32x4 o25 = mfma(SEL1, Y22, o25_b); SB();
      const unsigned k22_0 = pk(o22[0], o22[1]); SB();
      const unsigned k22_1 = pk(o22[2], o22[3]); SB();
      const unsigned k23_0 = pk(o23[0], o23[1]); SB();
      const unsigned k23_1 = pk(o23[2], o23[3]); SB();
      bstore(ors, u32x4{k22_0, k22_1, k23_0, k23_1}, so2 + 64); SB();
      const unsigned k24_0 = pk(o24[0], o24[1]); SB();
      const unsigned k24_1 = pk(o24[2], o24[3]); SB();
      const unsigned k25_0 = pk(o25[0], o25[1]); SB();
      const unsigned k25_1 = pk(o25[2], o25[3]); SB();
      bstore(ors, u32x4{k24_0, k24_1, k25_0, k25_1}, so2 + 128); SB();
    }
  }
#undef flag_arrive
  asm volatile(".Lvs_end:");
}

}

extern "C" void kernel_launch(void* const* d_in, const int* in_sizes, int n_in, void* d_out, int out_size,
                              void* d_ws, size_t ws_size, hipStream_t stream) {
  vs::swin_kernel<<<256, 512, 0, stream>>>((const char*)d_in[0], (const char*)d_in[1], (const char*)d_in[2],
                                           (const char*)d_in[3], (const char*)d_in[4], (char*)d_out);
}
```
